```python
import torch

def _softmax(x, axis=-1):
    return torch.softmax(x, dim=axis)
def _gelu(x):
    return torch.nn.functional.gelu(x, approximate="tanh")
def _silu(x):
    return torch.nn.functional.silu(x)
def _sigmoid(x):
    return torch.sigmoid(x)
def _layernorm(x, axis=-1, eps=1e-5):
    m = x.mean(dim=axis, keepdim=True)
    v = x.var(dim=axis, keepdim=True, unbiased=False)
    return (x - m) / torch.sqrt(v + eps)
def _scatter_add(tgt, idx, src, axis=0):
    return tgt.clone().index_add_(axis, idx, src)
def _segment_sum(src, seg, n):
    out = torch.zeros((n,) + src.shape[1:], dtype=src.dtype)
    return out.index_add_(0, seg, src)
def _pad(x, pw):
    flat = tuple(v for pair in reversed(pw) for v in pair)
    return torch.nn.functional.pad(x, flat)

kernel_name = 'standalone__se_block__vision'

def setup_inputs():

    _w = (lambda a: (lambda t: t.to(torch.bfloat16) if t.is_floating_point() else t)(torch.as_tensor(a)))
    H = W = 512
    C = 192; r = max(C // 16, 4)
    return {
        "x": _w((lambda *s: torch.randn(*s, dtype=torch.bfloat16) * 3.2)(1, H, W, C)),
        "w_down": _w((lambda *s: torch.randn(*s, dtype=torch.bfloat16) * 3.2)(C, r)),
        "w_up": _w((lambda *s: torch.randn(*s, dtype=torch.bfloat16) * 3.2)(r, C)),
    }

def reference(x, w_down, w_up):
    s = x.mean(axis=(1, 2))
    s = _sigmoid(_silu(s @ w_down) @ w_up)
    return x * s[:, None, None, :]
```

```cpp
#include <hip/hip_runtime.h>
#include <hip/hip_bf16.h>
#include <stdint.h>

#define AUX_LD 2
#define AUX_ST 2
#define NREP 8
#define MSTRIDE 16
#define INTERLEAVE 0
#define STAMP 0

namespace {

#define GAS __attribute__((address_space(1)))
#define LAS __attribute__((address_space(3)))
typedef unsigned v4u __attribute__((ext_vector_type(4)));
typedef unsigned v2u __attribute__((ext_vector_type(2)));
typedef float f32x4 __attribute__((ext_vector_type(4)));
typedef float f32x2 __attribute__((ext_vector_type(2)));
typedef short s16x8 __attribute__((ext_vector_type(8)));
typedef short s16x4 __attribute__((ext_vector_type(4)));
typedef __bf16 bf16x8 __attribute__((ext_vector_type(8)));
typedef __bf16 bf16x2 __attribute__((ext_vector_type(2)));

constexpr int C = 192, R = 12;
constexpr int HW = 512 * 512;
constexpr int NWG = 256, NT = 768, NSTEP = 32;
constexpr unsigned XBYTES = (unsigned)HW * C * 2;
constexpr unsigned STEP = NT * 16;
constexpr unsigned SLAB = STEP * NSTEP;
static_assert(SLAB * NWG == XBYTES, "x is dealt out exactly");
constexpr int NRED = C / 2;
constexpr int RSRC_W3 = 0x00020000;
constexpr int AUX_SC1 = 16;

constexpr unsigned WS_EPOCH = 0;
constexpr unsigned WS_PART = 4096;
constexpr unsigned PART_ROW = NRED * 16;
constexpr unsigned WS_MEAN = WS_PART + NWG * PART_ROW;
constexpr unsigned MEAN_REP = NRED * MSTRIDE;
constexpr unsigned WS_STAMP = WS_MEAN + NREP * MEAN_REP;
constexpr unsigned WS_BYTES = WS_STAMP + NWG * 16 * 8;
constexpr unsigned kSpinCap = 1u << 19;

constexpr int L_RAW = 0;
constexpr int L_WD = 9216;
constexpr int L_WU = L_WD + 6144;
constexpr int L_PART = L_WU + 6144;
constexpr int L_MEAN = L_PART + 3072;
constexpr int L_SCALE = L_MEAN + 384;
constexpr int L_BYTES = L_SCALE + 768;

__device__ __forceinline__ float bf_lo(unsigned u) { return __builtin_bit_cast(float, u << 16); }
__device__ __forceinline__ float bf_hi(unsigned u) { return __builtin_bit_cast(float, u & 0xffff0000u); }
__device__ __forceinline__ unsigned pack_bf(float lo, float hi) {
    const bf16x2 p = {(__bf16)lo, (__bf16)hi};
    return __builtin_bit_cast(unsigned, p);
}
__device__ __forceinline__ float round_bf(float x) { return bf_lo(pack_bf(x, 0.f)); }
__device__ __forceinline__ float f_of(unsigned u) { return __builtin_bit_cast(float, u); }
__device__ __forceinline__ unsigned u_of(float f) { return __builtin_bit_cast(unsigned, f); }

__device__ __forceinline__ float fold32(float a, float b) {
    const auto r = __builtin_amdgcn_permlane32_swap(u_of(a), u_of(b), false, false);
    return f_of(r[0]) + f_of(r[1]);
}
__device__ __forceinline__ float fold16(float a, float b) {
    const auto r = __builtin_amdgcn_permlane16_swap(u_of(a), u_of(b), false, false);
    return f_of(r[0]) + f_of(r[1]);
}
template <int N> __device__ __forceinline__ float add_ror(float x) {
    return x + f_of(__builtin_amdgcn_update_dpp(0u, u_of(x), 0x120 + N, 0xf, 0xf, true));
}

__device__ __forceinline__ v4u make_granule(unsigned v0, unsigned v1, unsigned ta, unsigned tb) {
    return v4u{v0, ta ^ v1, v1, tb ^ v0};
}
__device__ __forceinline__ bool granule_ok(v4u g, unsigned ta, unsigned tb) {
    return ((g.y ^ g.z) == ta) & ((g.w ^ g.x) == tb);
}

#define STAMP_AT(i) do { } while (0)

__global__ void __launch_bounds__(NT, 3)
se_block_kernel(const unsigned short* __restrict__ x, const unsigned short* __restrict__ wd,
                const unsigned short* __restrict__ wu, unsigned short* __restrict__ out, unsigned char* __restrict__ ws) {
    __shared__ __attribute__((aligned(16))) unsigned char smem[L_BYTES];
    LAS unsigned char* L = (LAS unsigned char*)smem;
    const int tid = threadIdx.x, lane = tid & 63, w = __builtin_amdgcn_readfirstlane(tid >> 6), b = blockIdx.x;
    const int seg = w % 3, pb = w / 3;

    const __amdgpu_buffer_rsrc_t rs_ws = __builtin_amdgcn_make_buffer_rsrc(ws, 0, (int)WS_BYTES, RSRC_W3);
    const __amdgpu_buffer_rsrc_t rs_x = __builtin_amdgcn_make_buffer_rsrc((unsigned char*)x + (size_t)b * SLAB, 0, (int)SLAB, RSRC_W3);
    const __amdgpu_buffer_rsrc_t rs_o = __builtin_amdgcn_make_buffer_rsrc((unsigned char*)out + (size_t)b * SLAB, 0, (int)SLAB, RSRC_W3);
    const unsigned voff = (unsigned)((pb * 8 + (lane >> 3)) * 384 + seg * 128 + (lane & 7) * 16);
    constexpr unsigned JSTR = STEP;

    const v2u ep = __builtin_amdgcn_raw_buffer_load_b64(rs_ws, WS_EPOCH, 0, AUX_SC1);
    v4u wraw = {0u, 0u, 0u, 0u};
    if (tid < 288) wraw = *(const GAS v4u*)((const GAS unsigned char*)wd + tid * 16);
    else if (tid < 576) wraw = *(const GAS v4u*)((const GAS unsigned char*)wu + (tid - 288) * 16);
    __builtin_amdgcn_sched_barrier(0);
    v4u d[NSTEP];
#pragma unroll
    for (int j = 0; j < NSTEP; ++j) d[j] = __builtin_amdgcn_raw_buffer_load_b128(rs_x, voff, j * JSTR, AUX_LD);
    __builtin_amdgcn_sched_barrier(0);

    const unsigned long long tag = (((unsigned long long)ep.y << 32) | ep.x) + 1ull;
    const unsigned ta = __builtin_amdgcn_readfirstlane((unsigned)tag), tb = __builtin_amdgcn_readfirstlane((unsigned)(tag >> 32));

    if (tid < 576) *(LAS v4u*)(L + L_RAW + tid * 16) = wraw;
    __syncthreads();
    {
        const LAS unsigned short* rwd = (const LAS unsigned short*)(L + L_RAW);
        const LAS unsigned short* rwu = rwd + C * R;
        const int f = tid >> 1, ks = f >> 6, l = f & 63, j = l & 15, k0 = ks * 32 + 8 * (l >> 4) + (tid & 1) * 4;
        unsigned a[4];
#pragma unroll
        for (int i = 0; i < 4; ++i) a[i] = j < R ? rwd[(k0 + i) * R + j] : 0;
        *(LAS v2u*)(L + L_WD + tid * 8) = v2u{a[0] | (a[1] << 16), a[2] | (a[3] << 16)};
        const int t = tid >> 6, q = lane >> 4;
#pragma unroll
        for (int i = 0; i < 4; ++i) a[i] = q < 3 ? rwu[(4 * q + i) * C + 16 * t + (lane & 15)] : 0;
        *(LAS v2u*)(L + L_WU + tid * 8) = v2u{a[0] | (a[1] << 16), a[2] | (a[3] << 16)};
    }
    __builtin_amdgcn_sched_barrier(0);

    float acc[8];
#pragma unroll
    for (int e = 0; e < 8; ++e) acc[e] = 0.f;
#pragma unroll
    for (int j = 0; j < NSTEP; ++j) {
#pragma unroll
        for (int k = 0; k < 4; ++k) {
            acc[2 * k] += bf_lo(d[j][k]);
            acc[2 * k + 1] += bf_hi(d[j][k]);
        }
    }
    if (w == 0) STAMP_AT(1);
    {
        const float u0 = fold32(acc[0], acc[4]), u1 = fold32(acc[1], acc[5]), u2 = fold32(acc[2], acc[6]), u3 = fold32(acc[3], acc[7]);
        const float v0 = fold16(u0, u2), v1 = fold16(u1, u3);
        const float t0 = add_ror<8>(v0), t1 = add_ror<8>(v1);
        const float r = (lane & 8) ? t1 : t0;
        const int ch = seg * 64 + (lane & 7) * 8 + ((lane >> 3) & 1) + 2 * ((lane >> 4) & 1) + 4 * (lane >> 5);
        *(LAS float*)(L + L_PART + (pb * C + ch) * 4) = r;
    }
    __syncthreads();
    if (w == 0) STAMP_AT(2);

    bool good = true;
    if (w < 2) {
        if (tid < NRED) {
            const LAS f32x2* p = (const LAS f32x2*)(L + L_PART) + tid;
            const f32x2 s = (p[0] + p[C / 2]) + (p[C] + p[3 * C / 2]);
            __builtin_amdgcn_raw_buffer_store_b128(make_granule(u_of(s.x), u_of(s.y), ta, tb), rs_ws,
                                                   WS_PART + b * PART_ROW + tid * 16, 0, AUX_SC1);
        }
        if (w == 0) STAMP_AT(3);
    } else if (w == 2) {
        if (b < NRED) {
            v4u g[4];
            const unsigned off = WS_PART + lane * PART_ROW + b * 16;
            for (unsigned spins = 0;; ++spins) {
                asm volatile("" ::: "memory");
                bool ok = true;
#pragma unroll
                for (int k = 0; k < 4; ++k) g[k] = __builtin_amdgcn_raw_buffer_load_b128(rs_ws, off, k * 64 * PART_ROW, AUX_SC1);
#pragma unroll
                for (int k = 0; k < 4; ++k) ok &= granule_ok(g[k], ta, tb);
                if (__all(ok)) break;
                if (spins > kSpinCap) { good = false; break; }
            }
            STAMP_AT(4);
            const float a = (f_of(g[0].x) + f_of(g[1].x)) + (f_of(g[2].x) + f_of(g[3].x));
            const float c = (f_of(g[0].z) + f_of(g[1].z)) + (f_of(g[2].z) + f_of(g[3].z));
            float s = fold32(a, c);
            s = fold16(s, s);
            s = add_ror<8>(s); s = add_ror<4>(s); s = add_ror<2>(s); s = add_ror<1>(s);
            const float m = good ? s * (1.0f / HW) : __builtin_nanf("");
            const float m0 = f_of(__builtin_amdgcn_readlane(u_of(m), 0)), m1 = f_of(__builtin_amdgcn_readlane(u_of(m), 32));
            if (lane < NREP)
                __builtin_amdgcn_raw_buffer_store_b128(make_granule(pack_bf(m0, m1), 0u, ta, tb), rs_ws,
                                                       WS_MEAN + lane * MEAN_REP + b * MSTRIDE, 0, AUX_SC1);
            STAMP_AT(5);
        }
    } else if (w == 3) {
        const unsigned off = WS_MEAN + (b % NREP) * MEAN_REP + lane * MSTRIDE;
        const unsigned off2 = lane < 32 ? off + 64 * MSTRIDE : off;
        v4u g0, g1;
        for (unsigned spins = 0;; ++spins) {
            asm volatile("" ::: "memory");
            g0 = __builtin_amdgcn_raw_buffer_load_b128(rs_ws, off, 0, AUX_SC1);
            g1 = __builtin_amdgcn_raw_buffer_load_b128(rs_ws, off2, 0, AUX_SC1);
            if (__all(granule_ok(g0, ta, tb) & granule_ok(g1, ta, tb))) break;
            if (spins > kSpinCap) { good = false; break; }
        }
        STAMP_AT(6);
        if (b == 0 && lane == 0) __builtin_amdgcn_raw_buffer_store_b64(v2u{ta, tb}, rs_ws, WS_EPOCH, 0, AUX_SC1);
        *(LAS unsigned*)(L + L_MEAN + lane * 4) = g0.x;
        if (lane < 32) *(LAS unsigned*)(L + L_MEAN + 256 + lane * 4) = g1.x;
        f32x4 h = {0.f, 0.f, 0.f, 0.f};
#pragma unroll
        for (int ks = 0; ks < 6; ++ks) {
            const v4u a = *(const LAS v4u*)(L + L_WD + (ks * 64 + lane) * 16);
            const v4u m = *(const LAS v4u*)(L + L_MEAN + (ks * 32 + 8 * (lane >> 4)) * 2);
            h = __builtin_amdgcn_mfma_f32_16x16x32_bf16(__builtin_bit_cast(bf16x8, a), __builtin_bit_cast(bf16x8, m), h, 0, 0, 0);
        }
        float hs[4];
#pragma unroll
        for (int i = 0; i < 4; ++i) {
            const float v = round_bf(h[i]);
            hs[i] = v * __builtin_amdgcn_rcpf(1.0f + __expf(-v));
        }
        const v2u hp = {pack_bf(hs[0], hs[1]), pack_bf(hs[2], hs[3])};
        const int q = lane >> 4;
        float zz[3];
#pragma unroll
        for (int i = 0; i < 3; ++i) {
            float z[4];
#pragma unroll
            for (int t = 0; t < 4; ++t) {
                const v2u bw = *(const LAS v2u*)(L + L_WU + ((4 * i + t) * 64 + lane) * 8);
                const f32x4 zero = {0.f, 0.f, 0.f, 0.f};
                z[t] = __builtin_amdgcn_mfma_f32_16x16x16bf16_1k(__builtin_bit_cast(s16x4, hp), __builtin_bit_cast(s16x4, bw), zero, 0, 0, 0)[0];
            }
            zz[i] = q == 0 ? z[0] : q == 1 ? z[1] : q == 2 ? z[2] : z[3];
            __builtin_amdgcn_sched_barrier(0);
        }
#pragma unroll
        for (int i = 0; i < 3; ++i) {
            const float v = round_bf(zz[i]);
            const float s = round_bf(__builtin_amdgcn_rcpf(1.0f + __expf(-v)));
            *(LAS float*)(L + L_SCALE + (64 * i + lane) * 4) = good ? s : __builtin_nanf("");
        }
        STAMP_AT(7);
    }
    __syncthreads();
    if (w == 0) STAMP_AT(8);

    const f32x4 s0 = *(const LAS f32x4*)(L + L_SCALE + (seg * 64 + (lane & 7) * 8) * 4);
    const f32x4 s1 = *(const LAS f32x4*)(L + L_SCALE + (seg * 64 + (lane & 7) * 8) * 4 + 16);
#pragma unroll
    for (int j = 0; j < NSTEP; ++j) {
        asm volatile("" : "+v"(d[j]));
        v4u o;
        o.x = pack_bf(bf_lo(d[j].x) * s0.x, bf_hi(d[j].x) * s0.y);
        o.y = pack_bf(bf_lo(d[j].y) * s0.z, bf_hi(d[j].y) * s0.w);
        o.z = pack_bf(bf_lo(d[j].z) * s1.x, bf_hi(d[j].z) * s1.y);
        o.w = pack_bf(bf_lo(d[j].w) * s1.z, bf_hi(d[j].w) * s1.w);
        __builtin_amdgcn_raw_buffer_store_b128(o, rs_o, voff, j * JSTR, AUX_ST);
    }
}

}

extern "C" void kernel_launch(void* const* d_in, const int* in_sizes, int n_in, void* d_out, int out_size, void* d_ws,
                              size_t ws_size, hipStream_t stream) {
    (void)in_sizes; (void)n_in; (void)out_size; (void)ws_size;
    se_block_kernel<<<NWG, NT, 0, stream>>>((const unsigned short*)d_in[0], (const unsigned short*)d_in[1],
                                            (const unsigned short*)d_in[2], (unsigned short*)d_out, (unsigned char*)d_ws);
}
```

```cpp
#include <hip/hip_runtime.h>
#include <hip/hip_bf16.h>
#include <stdint.h>

#define AUX_LD 2
#define AUX_ST 16
#define NREP 8
#define MSTRIDE 32
#define INTERLEAVE 1
#define LDWIN 6
#define PACE_LD 0
#define PACE_ST 38
#define ST_POL "sc1"
#define STAMP 0
#define DIAG 0

namespace {

#define GAS __attribute__((address_space(1)))
#define LAS __attribute__((address_space(3)))
typedef unsigned v4u __attribute__((ext_vector_type(4)));
typedef unsigned v2u __attribute__((ext_vector_type(2)));
typedef unsigned v32u __attribute__((ext_vector_type(32)));
typedef float f32x4 __attribute__((ext_vector_type(4)));
typedef float f32x2 __attribute__((ext_vector_type(2)));
typedef float f32x8 __attribute__((ext_vector_type(8)));
typedef short s16x4 __attribute__((ext_vector_type(4)));
typedef __bf16 bf16x8 __attribute__((ext_vector_type(8)));
typedef __bf16 bf16x2 __attribute__((ext_vector_type(2)));

constexpr int C = 192, R = 12;
constexpr int HW = 512 * 512;
constexpr int NWG = 256, NT = 768, NSTEP = 32;
constexpr unsigned XBYTES = (unsigned)HW * C * 2;
constexpr unsigned STEP = NT * 16;
constexpr unsigned SLAB = STEP * NSTEP;
static_assert(SLAB * NWG == XBYTES, "x is dealt out exactly");
constexpr unsigned JSTR = INTERLEAVE ? STEP * NWG : STEP;
constexpr int NRED = C / 2;
constexpr int RSRC_W3 = 0x00020000;
constexpr int AUX_SC1 = 16;

constexpr unsigned WS_EPOCH = 0;
constexpr unsigned WS_PART = 4096;
constexpr unsigned PART_ROW = NWG * 16 + 128;
constexpr unsigned WS_MEAN = WS_PART + NRED * PART_ROW;
constexpr unsigned MEAN_REP = NRED * MSTRIDE;
constexpr unsigned WS_STAMP = WS_MEAN + NREP * MEAN_REP;
constexpr unsigned WS_BYTES = WS_STAMP + (STAMP ? NWG * 16 * 8 : 0);
constexpr unsigned kSpinCap = 1u << 19;

constexpr int L_WD = 0;
constexpr int L_WU = L_WD + C * 32;
constexpr int L_PART = L_WU + 16 * C * 2;
constexpr int L_MEAN = L_PART + 4 * C * 4;
constexpr int L_SCALE = L_MEAN + C * 2;
constexpr int L_RED = L_SCALE + C * 4;
constexpr int L_CNT = L_RED + 32;
constexpr int L_BYTES = L_CNT + 16;

__device__ __forceinline__ float bf_lo(unsigned u) { return __builtin_bit_cast(float, u << 16); }
__device__ __forceinline__ float bf_hi(unsigned u) { return __builtin_bit_cast(float, u & 0xffff0000u); }
__device__ __forceinline__ unsigned pack_bf(float lo, float hi) {
    const bf16x2 p = {(__bf16)lo, (__bf16)hi};
    return __builtin_bit_cast(unsigned, p);
}
__device__ __forceinline__ float round_bf(float x) { return bf_lo(pack_bf(x, 0.f)); }
__device__ __forceinline__ float f_of(unsigned u) { return __builtin_bit_cast(float, u); }
__device__ __forceinline__ unsigned u_of(float f) { return __builtin_bit_cast(unsigned, f); }

__device__ __forceinline__ float fold32(float a, float b) {
    const auto r = __builtin_amdgcn_permlane32_swap(u_of(a), u_of(b), false, false);
    return f_of(r[0]) + f_of(r[1]);
}
__device__ __forceinline__ float fold16(float a, float b) {
    const auto r = __builtin_amdgcn_permlane16_swap(u_of(a), u_of(b), false, false);
    return f_of(r[0]) + f_of(r[1]);
}
template <int N> __device__ __forceinline__ float add_ror(float x) {
    return x + f_of(__builtin_amdgcn_update_dpp(0u, u_of(x), 0x120 + N, 0xf, 0xf, true));
}

__device__ __forceinline__ v4u make_granule(unsigned v0, unsigned v1, unsigned ta, unsigned tb) {
    return v4u{v0, ta ^ v1, v1, tb ^ v0};
}
__device__ __forceinline__ bool granule_ok(v4u g, unsigned ta, unsigned tb) {
    return ((g.y ^ g.z) == ta) & ((g.w ^ g.x) == tb);
}

__device__ __forceinline__ bool arrive_last(LAS unsigned char* cnt, unsigned n, int lane) {
    unsigned old = 0;
    __builtin_amdgcn_fence(__ATOMIC_RELEASE, "workgroup");
    if (lane == 0) old = __hip_atomic_fetch_add((LAS unsigned*)cnt, 1u, __ATOMIC_RELAXED, __HIP_MEMORY_SCOPE_WORKGROUP);
    old = __builtin_amdgcn_readfirstlane(old);
    __builtin_amdgcn_fence(__ATOMIC_ACQUIRE, "workgroup");
    return old == n - 1;
}
__device__ __forceinline__ void wait_arrivals(LAS unsigned char* cnt, unsigned n) {
    for (unsigned spins = 0; spins < kSpinCap; ++spins) {
        if (__hip_atomic_load((LAS unsigned*)cnt, __ATOMIC_RELAXED, __HIP_MEMORY_SCOPE_WORKGROUP) >= n) break;
        __builtin_amdgcn_s_sleep(1);
    }
}
__device__ __forceinline__ void pace(unsigned t0, unsigned ticks) {
    while ((unsigned)__builtin_amdgcn_s_memrealtime() - t0 < ticks) __builtin_amdgcn_s_sleep(1);
}
__device__ __forceinline__ v2u read_tr(LAS unsigned char* p) {
    return __builtin_bit_cast(v2u, __builtin_amdgcn_ds_read_tr16_b64_v4i16((LAS s16x4*)p));
}

#define STAMP_AT(i) do { } while (0)

__global__ void __launch_bounds__(NT, 3)
se_block_kernel(const unsigned short* __restrict__ x, const unsigned short* __restrict__ wd,
                const unsigned short* __restrict__ wu, unsigned short* __restrict__ out, unsigned char* __restrict__ ws) {
    __shared__ __attribute__((aligned(16))) unsigned char smem[L_BYTES];
    LAS unsigned char* L = (LAS unsigned char*)smem;
    const int tid = threadIdx.x, lane = tid & 63, w = __builtin_amdgcn_readfirstlane(tid >> 6), b = blockIdx.x;
    const int seg = w % 3, pb = w / 3;
    const unsigned t_ld = PACE_LD ? (unsigned)__builtin_amdgcn_s_memrealtime() : 0u;

    const __amdgpu_buffer_rsrc_t rs_ws = __builtin_amdgcn_make_buffer_rsrc(ws, 0, (int)WS_BYTES, RSRC_W3);
    const size_t base = INTERLEAVE ? 0 : (size_t)b * SLAB;
    const unsigned bytes = INTERLEAVE ? XBYTES : SLAB;
    const __amdgpu_buffer_rsrc_t rs_x = __builtin_amdgcn_make_buffer_rsrc((unsigned char*)x + base, 0, (int)bytes, RSRC_W3);
    const unsigned voff = (INTERLEAVE ? (unsigned)b * STEP : 0u) + (unsigned)((pb * 8 + (lane >> 3)) * 384 + seg * 128 + (lane & 7) * 16);

    const v2u ep = __builtin_amdgcn_raw_buffer_load_b64(rs_ws, WS_EPOCH, 0, AUX_SC1);
    const int wt = tid < 576 ? tid : tid - 576;
    const v4u wraw = *(const GAS v4u*)(wt < 288 ? (const GAS unsigned char*)wd + wt * 16 : (const GAS unsigned char*)wu + (wt - 288) * 16);
    __builtin_amdgcn_sched_barrier(0);
    v32u D[4];
    v4u d[NSTEP];
#pragma unroll
    for (int j = 0; j < LDWIN; ++j) d[j] = __builtin_amdgcn_raw_buffer_load_b128(rs_x, voff, j * JSTR, AUX_LD);
    __builtin_amdgcn_sched_barrier(0);

    if (tid == 0) *(LAS v2u*)(L + L_CNT) = v2u{0u, 0u};
    __syncthreads();

    {
        int a0, a1;
        v2u p0 = {wraw.x, wraw.y}, p1 = {wraw.z, wraw.w};
        if (tid < 288) {
            const int g0 = 2 * tid, g1 = g0 + 1;
            a0 = L_WD + (g0 / 3) * 32 + (g0 % 3) * 8;
            a1 = L_WD + (g1 / 3) * 32 + (g1 % 3) * 8;
        } else if (tid < 576) {
            a0 = L_WU + (tid - 288) * 16;
            a1 = a0 + 8;
        } else {
            a0 = L_WD + (tid - 576) * 32 + 24;
            a1 = L_WU + R * C * 2 + (tid - 576) * 8;
            p0 = p1 = v2u{0u, 0u};
        }
        *(LAS v2u*)(L + a0) = p0;
        *(LAS v2u*)(L + a1) = p1;
    }
    const unsigned long long tag = (((unsigned long long)ep.y << 32) | ep.x) + 1ull;
    const unsigned ta = __builtin_amdgcn_readfirstlane((unsigned)tag), tb = __builtin_amdgcn_readfirstlane((unsigned)(tag >> 32));

    float acc[8];
#pragma unroll
    for (int e = 0; e < 8; ++e) acc[e] = 0.f;
#pragma unroll
    for (int j = 0; j < NSTEP; ++j) {
#pragma unroll
        for (int k = 0; k < 4; ++k) {
            acc[2 * k] += bf_lo(d[j][k]);
            acc[2 * k + 1] += bf_hi(d[j][k]);
            D[j / 8][4 * (j % 8) + k] = d[j][k];
        }
        if (j + LDWIN < NSTEP) {
            if (PACE_LD) pace(t_ld, (j + LDWIN) * PACE_LD / 10);
            d[j + LDWIN] = __builtin_amdgcn_raw_buffer_load_b128(rs_x, voff, (j + LDWIN) * JSTR, AUX_LD);
            __builtin_amdgcn_sched_barrier(0);
        }
    }
    {
        const float u0 = fold32(acc[0], acc[4]), u1 = fold32(acc[1], acc[5]), u2 = fold32(acc[2], acc[6]), u3 = fold32(acc[3], acc[7]);
        const float v0 = fold16(u0, u2), v1 = fold16(u1, u3);
        const float t0 = add_ror<8>(v0), t1 = add_ror<8>(v1);
        const float r = (lane & 8) ? t1 : t0;
        const int ch = seg * 64 + (lane & 7) * 8 + ((lane >> 3) & 1) + 2 * ((lane >> 4) & 1) + 4 * (lane >> 5);
        *(LAS float*)(L + L_PART + (pb * C + ch) * 4) = r;
    }
    bool good = true;
    if (arrive_last(L + L_CNT, NT / 64, lane)) {
        STAMP_AT(2);
        const LAS f32x2* p = (const LAS f32x2*)(L + L_PART) + lane;
        const LAS f32x2* q = (const LAS f32x2*)(L + L_PART) + 64 + (lane & 31);
        const f32x2 s0 = (p[0] + p[C / 2]) + (p[C] + p[3 * C / 2]);
        const f32x2 s1 = (q[0] + q[C / 2]) + (q[C] + q[3 * C / 2]);
        const unsigned off = WS_PART + lane * PART_ROW + b * 16;
        __builtin_amdgcn_raw_buffer_store_b128(make_granule(u_of(s0.x), u_of(s0.y), ta, tb), rs_ws, off, 0, AUX_SC1);
        if (lane < 32)
            __builtin_amdgcn_raw_buffer_store_b128(make_granule(u_of(s1.x), u_of(s1.y), ta, tb), rs_ws, off, 64 * PART_ROW, AUX_SC1);
        if (lane == 0) __hip_atomic_fetch_add((LAS unsigned*)(L + L_CNT), 1u, __ATOMIC_RELAXED, __HIP_MEMORY_SCOPE_WORKGROUP);
        STAMP_AT(3);
    }
    if (w < 4) {
        if (b < NRED) {
            wait_arrivals(L + L_CNT, NT / 64 + 1);
            const unsigned off = WS_PART + b * PART_ROW + (w * 64 + lane) * 16;
            v4u g = {0u, 0u, 0u, 0u};
            bool k = false;
            for (unsigned spins = 0;; ++spins) {
                asm volatile("" ::: "memory");
                if (!k) g = __builtin_amdgcn_raw_buffer_load_b128(rs_ws, off, 0, AUX_SC1);
                k = granule_ok(g, ta, tb);
                if (__all(k)) break;
                if (spins > kSpinCap) { good = false; break; }
            }
            float s = fold32(f_of(g.x), f_of(g.z));
            s = fold16(s, s);
            s = add_ror<8>(s); s = add_ror<4>(s); s = add_ror<2>(s); s = add_ror<1>(s);
            if (!good) s = __builtin_nanf("");
            if ((lane & 31) == 0) *(LAS float*)(L + L_RED + w * 8 + (lane >> 5) * 4) = s;
            if (arrive_last(L + L_CNT + 4, 4, lane)) {
                STAMP_AT(4);
                const LAS f32x2* p = (const LAS f32x2*)(L + L_RED);
                const f32x2 m = ((p[0] + p[1]) + (p[2] + p[3])) * (1.0f / HW);
                if (lane < NREP)
                    __builtin_amdgcn_raw_buffer_store_b128(make_granule(pack_bf(m.x, m.y), 0u, ta, tb), rs_ws,
                                                           WS_MEAN + lane * MEAN_REP + b * MSTRIDE, 0, AUX_SC1);
                STAMP_AT(5);
            }
        }
    } else if (w == 4) {
        wait_arrivals(L + L_CNT, NT / 64 + 1);
        const unsigned off = WS_MEAN + (b % NREP) * MEAN_REP + lane * MSTRIDE;
        v4u g0 = {0u, 0u, 0u, 0u}, g1 = {0u, 0u, 0u, 0u};
        bool k0 = false, k1 = lane >= 32;
        for (unsigned spins = 0;; ++spins) {
            asm volatile("" ::: "memory");
            if (!k0) g0 = __builtin_amdgcn_raw_buffer_load_b128(rs_ws, off, 0, AUX_SC1);
            if (!k1) g1 = __builtin_amdgcn_raw_buffer_load_b128(rs_ws, off, 64 * MSTRIDE, AUX_SC1);
            k0 = granule_ok(g0, ta, tb); k1 = lane >= 32 || granule_ok(g1, ta, tb);
            if (__all(k0 & k1)) break;
            if (spins > kSpinCap) { good = false; break; }
        }
        STAMP_AT(6);
        if (b == 0 && lane == 0) __builtin_amdgcn_raw_buffer_store_b64(v2u{ta, tb}, rs_ws, WS_EPOCH, 0, AUX_SC1);
        *(LAS unsigned*)(L + L_MEAN + lane * 4) = g0.x;
        if (lane < 32) *(LAS unsigned*)(L + L_MEAN + 256 + lane * 4) = g1.x;
        const int q = lane >> 4, r4 = (lane >> 2) & 3, p4 = lane & 3;
        LAS unsigned char* pa = L + L_WD + (8 * q + r4) * 32 + p4 * 8;
        f32x4 h0 = {0.f, 0.f, 0.f, 0.f}, h1 = h0;
#pragma unroll
        for (int ks = 0; ks < 6; ++ks) {
            const v2u a0 = read_tr(pa + ks * 1024), a1 = read_tr(pa + ks * 1024 + 128);
            const v4u a = {a0.x, a0.y, a1.x, a1.y};
            const v4u m = *(const LAS v4u*)(L + L_MEAN + (ks * 32 + 8 * q) * 2);
            if (ks & 1) h1 = __builtin_amdgcn_mfma_f32_16x16x32_bf16(__builtin_bit_cast(bf16x8, a), __builtin_bit_cast(bf16x8, m), h1, 0, 0, 0);
            else h0 = __builtin_amdgcn_mfma_f32_16x16x32_bf16(__builtin_bit_cast(bf16x8, a), __builtin_bit_cast(bf16x8, m), h0, 0, 0, 0);
        }
        float hs[4];
#pragma unroll
        for (int i = 0; i < 4; ++i) {
            const float v = round_bf(h0[i] + h1[i]);
            hs[i] = v * __builtin_amdgcn_rcpf(1.0f + __expf(-v));
        }
        const v2u hp = {pack_bf(hs[0], hs[1]), pack_bf(hs[2], hs[3])};
        LAS unsigned char* pb2 = L + L_WU + (4 * q + r4) * (C * 2) + p4 * 8;
        float zz[3];
#pragma unroll
        for (int i = 0; i < 3; ++i) {
            float z[4];
#pragma unroll
            for (int t = 0; t < 4; ++t) {
                const v2u bw = read_tr(pb2 + (4 * i + t) * 32);
                const f32x4 zero = {0.f, 0.f, 0.f, 0.f};
                z[t] = __builtin_amdgcn_mfma_f32_16x16x16bf16_1k(__builtin_bit_cast(s16x4, hp), __builtin_bit_cast(s16x4, bw), zero, 0, 0, 0)[0];
            }
            zz[i] = q == 0 ? z[0] : q == 1 ? z[1] : q == 2 ? z[2] : z[3];
            __builtin_amdgcn_sched_barrier(0);
        }
#pragma unroll
        for (int i = 0; i < 3; ++i) {
            const float v = round_bf(zz[i]);
            const float s = round_bf(__builtin_amdgcn_rcpf(1.0f + __expf(-v)));
            *(LAS float*)(L + L_SCALE + (64 * i + lane) * 4) = good ? s : __builtin_nanf("");
        }
        STAMP_AT(7);
    }
    __syncthreads();
    if (w == 0) STAMP_AT(8);

    {
        const f32x4 s0 = *(const LAS f32x4*)(L + L_SCALE + (seg * 64 + (lane & 7) * 8) * 4);
        const f32x4 s1 = *(const LAS f32x4*)(L + L_SCALE + (seg * 64 + (lane & 7) * 8) * 4 + 16);
        const f32x8 sc = {s0.x, s0.y, s0.z, s0.w, s1.x, s1.y, s1.z, s1.w};
        const __amdgpu_buffer_rsrc_t rs_o = __builtin_amdgcn_make_buffer_rsrc((unsigned char*)out + base, 0, (int)bytes, RSRC_W3);
        asm volatile(
            "s_memrealtime s[40:41]\n\t"
            "s_mov_b32 s42, 0\n\t"
            "s_mov_b32 s43, 0\n\t"
            "s_mov_b32 s44, 0xffff0000\n\t"
            "s_mov_b32 s45, 0\n\t"
            "s_waitcnt lgkmcnt(0)\n"
            "1:\n\t"
            "s_set_gpr_idx_on s42, gpr_idx(SRC1)\n\t"
            "v_lshlrev_b32 v16, 16, v40\n\t"
            "v_and_b32 v17, s44, v40\n\t"
            "v_lshlrev_b32 v18, 16, v41\n\t"
            "v_and_b32 v19, s44, v41\n\t"
            "v_lshlrev_b32 v20, 16, v42\n\t"
            "v_and_b32 v21, s44, v42\n\t"
            "v_lshlrev_b32 v22, 16, v43\n\t"
            "v_and_b32 v23, s44, v43\n\t"
            "s_set_gpr_idx_off\n\t"
            "v_pk_mul_f32 v[16:17], v[16:17], v[8:9]\n\t"
            "v_pk_mul_f32 v[18:19], v[18:19], v[10:11]\n\t"
            "v_pk_mul_f32 v[20:21], v[20:21], v[12:13]\n\t"
            "v_pk_mul_f32 v[22:23], v[22:23], v[14:15]\n\t"
            "v_cvt_pk_bf16_f32 v24, v16, v17\n\t"
            "v_cvt_pk_bf16_f32 v25, v18, v19\n\t"
            "v_cvt_pk_bf16_f32 v26, v20, v21\n\t"
            "v_cvt_pk_bf16_f32 v27, v22, v23\n\t"
            ".if %c[dt]\n"
            "2:\n\t"
            "s_memrealtime s[46:47]\n\t"
            "s_waitcnt lgkmcnt(0)\n\t"
            "s_sub_u32 s46, s46, s40\n\t"
            "s_cmp_ge_u32 s46, s45\n\t"
            "s_cbranch_scc1 3f\n\t"
            "s_sleep 1\n\t"
            "s_branch 2b\n"
            "3:\n\t"
            ".endif\n\t"
            "buffer_store_dwordx4 v[24:27], %[voff], %[rs], s43 offen " ST_POL "\n\t"
            "s_add_u32 s43, s43, %[jstr]\n\t"
            "s_add_u32 s45, s45, %c[dt]\n\t"
            "s_add_u32 s42, s42, 4\n\t"
            "s_cmp_lt_u32 s42, 128\n\t"
            "s_cbranch_scc1 1b\n\t"
            :
            : "{v[40:71]}"(D[0]), "{v[72:103]}"(D[1]), "{v[104:135]}"(D[2]), "{v[136:167]}"(D[3]), "{v[8:15]}"(sc),
              [voff] "v"(voff), [rs] "s"(rs_o), [jstr] "i"(JSTR), [dt] "i"(PACE_ST)
            : "s40", "s41", "s42", "s43", "s44", "s45", "s46", "s47", "scc", "memory",
              "v16", "v17", "v18", "v19", "v20", "v21", "v22", "v23", "v24", "v25", "v26", "v27");
    }
}

}

extern "C" void kernel_launch(void* const* d_in, const int* in_sizes, int n_in, void* d_out, int out_size, void* d_ws,
                              size_t ws_size, hipStream_t stream) {
    (void)in_sizes; (void)n_in; (void)out_size; (void)ws_size;
    se_block_kernel<<<NWG, NT, 0, stream>>>((const unsigned short*)d_in[0], (const unsigned short*)d_in[1],
                                            (const unsigned short*)d_in[2], (unsigned short*)d_out, (unsigned char*)d_ws);
}
```

```cpp
#include <hip/hip_runtime.h>
#include <hip/hip_bf16.h>
#include <stdint.h>

#define AUX_LD 2
#define AUX_ST 16
#define NREP 8
#define MSTRIDE 64
#define INTERLEAVE 1
#define LDWIN 5
#define PACE_LD 410
#define PACE_ST 38
#define ST_POL "sc1"
#define H1DELAY 8
#define FORCE_FALLBACK 0
#define STAMP 0
#define DIAG 0

namespace {

#define GAS __attribute__((address_space(1)))
#define LAS __attribute__((address_space(3)))
typedef unsigned v4u __attribute__((ext_vector_type(4)));
typedef unsigned v2u __attribute__((ext_vector_type(2)));
typedef unsigned v32u __attribute__((ext_vector_type(32)));
typedef float f32x4 __attribute__((ext_vector_type(4)));
typedef float f32x2 __attribute__((ext_vector_type(2)));
typedef float f32x8 __attribute__((ext_vector_type(8)));
typedef short s16x4 __attribute__((ext_vector_type(4)));
typedef __bf16 bf16x8 __attribute__((ext_vector_type(8)));
typedef __bf16 bf16x2 __attribute__((ext_vector_type(2)));

constexpr int C = 192, R = 12;
constexpr int HW = 512 * 512;
constexpr int NWG = 256, NT = 768, NSTEP = 32;
constexpr unsigned XBYTES = (unsigned)HW * C * 2;
constexpr unsigned STEP = NT * 16;
constexpr unsigned SLAB = STEP * NSTEP;
static_assert(SLAB * NWG == XBYTES, "x is dealt out exactly");
constexpr unsigned JSTR = INTERLEAVE ? STEP * NWG : STEP;
constexpr int NRED = 24;
constexpr int NPOLL = 8;
constexpr int NMEAN = C / 4;
constexpr int RSRC_W3 = 0x00020000;
constexpr int AUX_SC1 = 16;

constexpr unsigned WS_EPOCH = 0;
constexpr unsigned WS_PART = 4096;
constexpr unsigned PART_ROW = NWG * 64 + 128;
constexpr unsigned WS_MEAN = WS_PART + NRED * PART_ROW;
constexpr unsigned MEAN_REP = NMEAN * MSTRIDE;
constexpr unsigned WS_STAMP = WS_MEAN + NREP * MEAN_REP;
constexpr unsigned WS_BYTES = WS_STAMP + (STAMP ? NWG * 16 * 8 : 0);
constexpr unsigned kSpinCap = 1u << 19;

constexpr int L_WD = 0;
constexpr int L_WU = L_WD + C * 32;
constexpr int L_PART = L_WU + 16 * C * 2;
constexpr int L_MEAN = L_PART + 4 * C * 4;
constexpr int L_SCALE = L_MEAN + C * 2;
constexpr int L_RED = L_SCALE + C * 4;
constexpr int L_CNT = L_RED + NPOLL * 32;
constexpr int L_BYTES = L_CNT + 16;

__device__ __forceinline__ float bf_lo(unsigned u) { return __builtin_bit_cast(float, u << 16); }
__device__ __forceinline__ float bf_hi(unsigned u) { return __builtin_bit_cast(float, u & 0xffff0000u); }
__device__ __forceinline__ unsigned pack_bf(float lo, float hi) {
    const bf16x2 p = {(__bf16)lo, (__bf16)hi};
    return __builtin_bit_cast(unsigned, p);
}
__device__ __forceinline__ float round_bf(float x) { return bf_lo(pack_bf(x, 0.f)); }
__device__ __forceinline__ float f_of(unsigned u) { return __builtin_bit_cast(float, u); }
__device__ __forceinline__ unsigned u_of(float f) { return __builtin_bit_cast(unsigned, f); }

__device__ __forceinline__ float fold32(float a, float b) {
    const auto r = __builtin_amdgcn_permlane32_swap(u_of(a), u_of(b), false, false);
    return f_of(r[0]) + f_of(r[1]);
}
__device__ __forceinline__ float fold16(float a, float b) {
    const auto r = __builtin_amdgcn_permlane16_swap(u_of(a), u_of(b), false, false);
    return f_of(r[0]) + f_of(r[1]);
}
template <int N> __device__ __forceinline__ float add_ror(float x) {
    return x + f_of(__builtin_amdgcn_update_dpp(0u, u_of(x), 0x120 + N, 0xf, 0xf, true));
}

__device__ __forceinline__ v4u make_granule(unsigned v0, unsigned v1, unsigned ta, unsigned tb) {
    return v4u{v0, v1, ta ^ v1, tb ^ v0};
}
__device__ __forceinline__ bool granule_ok(v4u g, unsigned ta, unsigned tb) {
    return ((g.z ^ g.y) == ta) & ((g.w ^ g.x) == tb);
}

__device__ __forceinline__ bool arrive_last(LAS unsigned char* cnt, unsigned n, int lane) {
    unsigned old = 0;
    __builtin_amdgcn_fence(__ATOMIC_RELEASE, "workgroup");
    if (lane == 0) old = __hip_atomic_fetch_add((LAS unsigned*)cnt, 1u, __ATOMIC_RELAXED, __HIP_MEMORY_SCOPE_WORKGROUP);
    old = __builtin_amdgcn_readfirstlane(old);
    __builtin_amdgcn_fence(__ATOMIC_ACQUIRE, "workgroup");
    return old == n - 1;
}
__device__ __forceinline__ void wait_arrivals(LAS unsigned char* cnt, unsigned n) {
    for (unsigned spins = 0; spins < kSpinCap; ++spins) {
        if (__hip_atomic_load((LAS unsigned*)cnt, __ATOMIC_RELAXED, __HIP_MEMORY_SCOPE_WORKGROUP) >= n) break;
        __builtin_amdgcn_s_sleep(1);
    }
}
__device__ __forceinline__ void pace(unsigned t0, unsigned ticks) {
    while ((unsigned)__builtin_amdgcn_s_memrealtime() - t0 < ticks) __builtin_amdgcn_s_sleep(1);
}
__device__ __forceinline__ v2u read_tr(LAS unsigned char* p) {
    return __builtin_bit_cast(v2u, __builtin_amdgcn_ds_read_tr16_b64_v4i16((LAS s16x4*)p));
}

#define STAMP_AT(i) do { } while (0)

__global__ void __launch_bounds__(NT, 3)
se_block_kernel(const unsigned short* __restrict__ x, const unsigned short* __restrict__ wd,
                const unsigned short* __restrict__ wu, unsigned short* __restrict__ out, unsigned char* __restrict__ ws) {
    __shared__ __attribute__((aligned(16))) unsigned char smem[L_BYTES];
    LAS unsigned char* L = (LAS unsigned char*)smem;
    const int tid = threadIdx.x, lane = tid & 63, w = __builtin_amdgcn_readfirstlane(tid >> 6), b = blockIdx.x;
    const int seg = w % 3, pb = w / 3;
    const unsigned long long t_start = PACE_LD ? __builtin_amdgcn_s_memrealtime() : 0ull;

    const __amdgpu_buffer_rsrc_t rs_ws = __builtin_amdgcn_make_buffer_rsrc(ws, 0, (int)WS_BYTES, RSRC_W3);
    const size_t base = INTERLEAVE ? 0 : (size_t)b * SLAB;
    const unsigned bytes = INTERLEAVE ? XBYTES : SLAB;
    const __amdgpu_buffer_rsrc_t rs_x = __builtin_amdgcn_make_buffer_rsrc((unsigned char*)x + base, 0, (int)bytes, RSRC_W3);
    const unsigned voff = (INTERLEAVE ? (unsigned)b * STEP : 0u) + (unsigned)((pb * 8 + (lane >> 3)) * 384 + seg * 128 + (lane & 7) * 16);

    v32u D[4];
    v4u d[NSTEP];
#pragma unroll
    for (int j = 0; j < LDWIN; ++j) d[j] = __builtin_amdgcn_raw_buffer_load_b128(rs_x, voff, j * JSTR, AUX_LD);
    __builtin_amdgcn_sched_barrier(0);
    const v2u ep = __builtin_amdgcn_raw_buffer_load_b64(rs_ws, WS_EPOCH, 0, AUX_SC1);
    const v4u wa = __builtin_amdgcn_raw_buffer_load_b128(__builtin_amdgcn_make_buffer_rsrc((void*)wd, 0, C * R * 2, RSRC_W3), tid * 16, 0, 0);
    const v4u wb = __builtin_amdgcn_raw_buffer_load_b128(__builtin_amdgcn_make_buffer_rsrc((void*)wu, 0, C * R * 2, RSRC_W3), (tid - 288) * 16, 0, 0);
    __builtin_amdgcn_sched_barrier(0);

    if (tid == 0) *(LAS v2u*)(L + L_CNT) = v2u{0u, 0u};

    asm volatile("" :: "s"(t_start));
    const unsigned t_ld = (unsigned)t_start;
    unsigned ta = 0, tb = 0;
    float acc[8];
#pragma unroll
    for (int e = 0; e < 8; ++e) acc[e] = 0.f;
#pragma unroll
    for (int j = 0; j < NSTEP; ++j) {
#pragma unroll
        for (int k = 0; k < 4; ++k) {
            acc[2 * k] += bf_lo(d[j][k]);
            acc[2 * k + 1] += bf_hi(d[j][k]);
            D[j / 8][4 * (j % 8) + k] = d[j][k];
        }
        if (j == LDWIN) {
            const v4u wv = wa | wb;
            const int g0 = 2 * tid, g1 = g0 + 1, t2 = tid - 288, t3 = tid - 576;
            const int a0 = tid < 288 ? L_WD + (g0 + ((g0 * 0x5556) >> 16)) * 8 : tid < 576 ? L_WU + t2 * 16 : L_WD + t3 * 32 + 24;
            const int a1 = tid < 288 ? L_WD + (g1 + ((g1 * 0x5556) >> 16)) * 8 : tid < 576 ? L_WU + t2 * 16 + 8 : L_WU + R * C * 2 + t3 * 8;
            *(LAS v2u*)(L + a0) = v2u{wv.x, wv.y};
            *(LAS v2u*)(L + a1) = v2u{wv.z, wv.w};
            unsigned long long tag = (((unsigned long long)ep.y << 32) | ep.x) + 1ull;
            if ((unsigned)tag == 0u) tag += 1ull;
            ta = __builtin_amdgcn_readfirstlane((unsigned)tag);
            tb = __builtin_amdgcn_readfirstlane((unsigned)(tag >> 32));
        }
        if (j == NSTEP / 2) __syncthreads();
        asm volatile("" : "+v"(acc[0]), "+v"(acc[1]), "+v"(acc[2]), "+v"(acc[3]), "+v"(acc[4]), "+v"(acc[5]), "+v"(acc[6]), "+v"(acc[7]));
        if (j + LDWIN < NSTEP) {
            if (PACE_LD) pace(t_ld, (j + LDWIN) * PACE_LD / 10);
            d[j + LDWIN] = __builtin_amdgcn_raw_buffer_load_b128(rs_x, voff, (j + LDWIN) * JSTR, AUX_LD);
            __builtin_amdgcn_sched_barrier(0);
        }
    }
    {
        const float u0 = fold32(acc[0], acc[4]), u1 = fold32(acc[1], acc[5]), u2 = fold32(acc[2], acc[6]), u3 = fold32(acc[3], acc[7]);
        const float v0 = fold16(u0, u2), v1 = fold16(u1, u3);
        const float t0 = add_ror<8>(v0), t1 = add_ror<8>(v1);
        const float r = (lane & 8) ? t1 : t0;
        const int ch = seg * 64 + (lane & 7) * 8 + ((lane >> 3) & 1) + 2 * ((lane >> 4) & 1) + 4 * (lane >> 5);
        *(LAS float*)(L + L_PART + (pb * C + ch) * 4) = r;
    }
    bool good = true;
    if (arrive_last(L + L_CNT, NT / 64, lane)) {
        STAMP_AT(2);
        const LAS f32x2* p = (const LAS f32x2*)(L + L_PART) + lane;
        const LAS f32x2* q = (const LAS f32x2*)(L + L_PART) + 64 + (lane & 31);
        const f32x2 s0 = (p[0] + p[C / 2]) + (p[C] + p[3 * C / 2]);
        const f32x2 s1 = (q[0] + q[C / 2]) + (q[C] + q[3 * C / 2]);
        const unsigned off = WS_PART + (lane >> 2) * PART_ROW + b * 64 + (lane & 3) * 16;
        __builtin_amdgcn_raw_buffer_store_b128(make_granule(u_of(s0.x), u_of(s0.y), ta, tb), rs_ws, off, 0, AUX_SC1);
        if (lane < 32)
            __builtin_amdgcn_raw_buffer_store_b128(make_granule(u_of(s1.x), u_of(s1.y), ta, tb), rs_ws, off, 16 * PART_ROW, AUX_SC1);
        if (lane == 0) __hip_atomic_fetch_add((LAS unsigned*)(L + L_CNT), 1u, __ATOMIC_RELAXED, __HIP_MEMORY_SCOPE_WORKGROUP);
        STAMP_AT(3);
    }
    if (w < NPOLL) {
        if (b < NRED) {
            wait_arrivals(L + L_CNT, NT / 64 + 1);
            if (H1DELAY) __builtin_amdgcn_s_sleep(H1DELAY);
            const unsigned off = WS_PART + b * PART_ROW + w * 2048 + lane * 16;
            v4u g0 = {0u, 0u, 0u, 0u}, g1 = {0u, 0u, 0u, 0u};
            bool k0 = false, k1 = false;
            for (unsigned spins = 0;; ++spins) {
                asm volatile("" ::: "memory");
                if (!k0) g0 = __builtin_amdgcn_raw_buffer_load_b128(rs_ws, off, 0, AUX_SC1);
                if (!k1) g1 = __builtin_amdgcn_raw_buffer_load_b128(rs_ws, off, 1024, AUX_SC1);
                k0 = granule_ok(g0, ta, tb); k1 = granule_ok(g1, ta, tb);
                if (__all(k0 & k1)) break;
                if (spins > kSpinCap) { good = false; break; }
            }
            float s = fold32(f_of(g0.x) + f_of(g1.x), f_of(g0.y) + f_of(g1.y));
            s = fold16(s, s);
            s = add_ror<8>(s); s = add_ror<4>(s);
            if (!good) s = __builtin_nanf("");
            if ((lane & 28) == 0) *(LAS float*)(L + L_RED + (w * 8 + 2 * (lane & 3) + (lane >> 5)) * 4) = s;
            if (arrive_last(L + L_CNT + 4, NPOLL, lane)) {
                STAMP_AT(4);
                const LAS f32x4* p = (const LAS f32x4*)(L + L_RED) + (lane & 1);
                const f32x4 m = (((p[0] + p[2]) + (p[4] + p[6])) + ((p[8] + p[10]) + (p[12] + p[14]))) * (1.0f / HW);
                if (lane < 2 * NREP)
                    __builtin_amdgcn_raw_buffer_store_b128(make_granule(pack_bf(m.x, m.y), pack_bf(m.z, m.w), ta, tb), rs_ws,
                                                           WS_MEAN + (lane >> 1) * MEAN_REP + (2 * b + (lane & 1)) * MSTRIDE, 0, AUX_SC1);
                STAMP_AT(5);
            }
        }
    } else if (w == NPOLL) {
        wait_arrivals(L + L_CNT, NT / 64 + 1);
        __builtin_amdgcn_fence(__ATOMIC_ACQUIRE, "workgroup");
        asm volatile(
            "v_lshrrev_b32 v2, 4, %[lane]\n\t"
            "v_bfe_u32 v3, %[lane], 2, 2\n\t"
            "v_and_b32 v4, 3, %[lane]\n\t"
            "v_lshlrev_b32 v34, 8, v2\n\t"
            "v_lshl_or_b32 v34, v3, 5, v34\n\t"
            "v_lshl_or_b32 v34, v4, 3, v34\n\t"
            "v_add_u32 v34, %[lds], v34\n\t"
            "v_and_b32 v35, 48, %[lane]\n\t"
            "v_add_u32 v35, %[lds], v35\n\t"
            "v_lshlrev_b32 v36, 3, %[lane]\n\t"
            "v_add_u32 v36, %[lds], v36\n\t"
            "v_lshrrev_b32 v5, 2, %[lane]\n\t"
            "v_mul_u32_u24 v37, 0x180, v5\n\t"
            "v_lshl_add_u32 v37, v4, 3, v37\n\t"
            "v_add_u32 v37, %[lds], v37\n\t"
            "v_cmp_gt_u32 vcc, %[nmean], %[lane]\n\t"
            "v_mul_u32_u24 v29, %[mstr], %[lane]\n\t"
            "s_nop 0\n\t"
            "v_cndmask_b32 v29, 0, v29, vcc\n\t"
            "v_add_u32 v29, %[moff], v29\n\t"
            "s_mov_b32 s48, 0\n\t"
            "s_mov_b32 s49, 0\n\t"
            "s_mov_b32 s50, 0xffff0000\n"
            "1:\n\t"
            "buffer_load_dwordx4 v[30:33], v29, %[rs], 0 offen sc1\n\t"
            "s_add_u32 s48, s48, 1\n\t"
            "s_waitcnt vmcnt(0)\n\t"
            "v_xor_b32 v2, v32, v31\n\t"
            "v_xor_b32 v3, v33, v30\n\t"
            "v_cmp_eq_u32 vcc, %[ta], v2\n\t"
            "v_cmp_eq_u32 s[52:53], %[tb], v3\n\t"
            "s_and_b64 vcc, vcc, s[52:53]\n\t"
            "s_cmp_eq_u64 vcc, exec\n\t"
            "s_cbranch_scc1 2f\n\t"
            "s_cmp_lt_u32 s48, %[cap]\n\t"
            "s_cbranch_scc1 1b\n\t"
            "s_mov_b32 s49, 1\n"
            "2:\n\t"
            "ds_write_b64 v36, v[30:31] offset:%c[lmean]\n\t"
            "ds_read_b64_tr_b16 v[2:3], v34 offset:%c[lwd]\n\t"
            "ds_read_b64_tr_b16 v[4:5], v34 offset:%c[lwd]+128\n\t"
            "ds_read_b128 v[6:9], v35 offset:%c[lmean]\n\t"
            "ds_read_b64_tr_b16 v[10:11], v34 offset:%c[lwd]+1024\n\t"
            "ds_read_b64_tr_b16 v[12:13], v34 offset:%c[lwd]+1152\n\t"
            "ds_read_b128 v[14:17], v35 offset:%c[lmean]+64\n\t"
            "ds_read_b64_tr_b16 v[18:19], v34 offset:%c[lwd]+2048\n\t"
            "ds_read_b64_tr_b16 v[20:21], v34 offset:%c[lwd]+2176\n\t"
            "ds_read_b128 v[22:25], v35 offset:%c[lmean]+128\n\t"
            "ds_read_b64_tr_b16 v[26:27], v34 offset:%c[lwd]+3072\n\t"
            "ds_read_b64_tr_b16 v[28:29], v34 offset:%c[lwd]+3200\n\t"
            "ds_read_b128 v[30:33], v35 offset:%c[lmean]+192\n\t"
            "s_waitcnt lgkmcnt(9)\n\t"
            "v_mfma_f32_16x16x32_bf16 v[2:5], v[2:5], v[6:9], 0\n\t"
            "ds_read_b64_tr_b16 v[6:7], v34 offset:%c[lwd]+4096\n\t"
            "ds_read_b64_tr_b16 v[8:9], v34 offset:%c[lwd]+4224\n\t"
            "s_waitcnt lgkmcnt(8)\n\t"
            "v_mfma_f32_16x16x32_bf16 v[2:5], v[10:13], v[14:17], v[2:5]\n\t"
            "ds_read_b128 v[10:13], v35 offset:%c[lmean]+256\n\t"
            "ds_read_b64_tr_b16 v[14:15], v34 offset:%c[lwd]+5120\n\t"
            "ds_read_b64_tr_b16 v[16:17], v34 offset:%c[lwd]+5248\n\t"
            "s_waitcnt lgkmcnt(8)\n\t"
            "v_mfma_f32_16x16x32_bf16 v[2:5], v[18:21], v[22:25], v[2:5]\n\t"
            "ds_read_b128 v[18:21], v35 offset:%c[lmean]+320\n\t"
            "s_waitcnt lgkmcnt(6)\n\t"
            "v_mfma_f32_16x16x32_bf16 v[2:5], v[26:29], v[30:33], v[2:5]\n\t"
            "s_waitcnt lgkmcnt(3)\n\t"
            "v_mfma_f32_16x16x32_bf16 v[2:5], v[6:9], v[10:13], v[2:5]\n\t"
            "s_waitcnt lgkmcnt(0)\n\t"
            "v_mfma_f32_16x16x32_bf16 v[2:5], v[14:17], v[18:21], v[2:5]\n\t"
            "ds_read_b64_tr_b16 v[6:7], v37 offset:%c[lwu]\n\t"
            "ds_read_b64_tr_b16 v[8:9], v37 offset:%c[lwu]+32\n\t"
            "ds_read_b64_tr_b16 v[10:11], v37 offset:%c[lwu]+64\n\t"
            "ds_read_b64_tr_b16 v[12:13], v37 offset:%c[lwu]+96\n\t"
            "ds_read_b64_tr_b16 v[22:23], v37 offset:%c[lwu]+256\n\t"
            "ds_read_b64_tr_b16 v[24:25], v37 offset:%c[lwu]+288\n\t"
            "ds_read_b64_tr_b16 v[26:27], v37 offset:%c[lwu]+320\n\t"
            "ds_read_b64_tr_b16 v[28:29], v37 offset:%c[lwu]+352\n\t"
            "ds_read_b64_tr_b16 v[14:15], v37 offset:%c[lwu]+128\n\t"
            "ds_read_b64_tr_b16 v[16:17], v37 offset:%c[lwu]+160\n\t"
            "ds_read_b64_tr_b16 v[18:19], v37 offset:%c[lwu]+192\n\t"
            "ds_read_b64_tr_b16 v[20:21], v37 offset:%c[lwu]+224\n\t"
            "v_cvt_pk_bf16_f32 v30, v2, v3\n\t"
            "v_cvt_pk_bf16_f32 v31, v4, v5\n\t"
            "v_lshlrev_b32 v2, 16, v30\n\t"
            "v_and_b32 v3, s50, v30\n\t"
            "v_lshlrev_b32 v4, 16, v31\n\t"
            "v_and_b32 v5, s50, v31\n\t"
            "v_mul_f32 v30, 0xbfb8aa3b, v2\n\t"
            "v_mul_f32 v31, 0xbfb8aa3b, v3\n\t"
            "v_mul_f32 v32, 0xbfb8aa3b, v4\n\t"
            "v_mul_f32 v33, 0xbfb8aa3b, v5\n\t"
            "v_exp_f32 v30, v30\n\t"
            "v_exp_f32 v31, v31\n\t"
            "v_exp_f32 v32, v32\n\t"
            "v_exp_f32 v33, v33\n\t"
            "v_add_f32 v30, 1.0, v30\n\t"
            "v_add_f32 v31, 1.0, v31\n\t"
            "v_add_f32 v32, 1.0, v32\n\t"
            "v_add_f32 v33, 1.0, v33\n\t"
            "v_rcp_f32 v30, v30\n\t"
            "v_rcp_f32 v31, v31\n\t"
            "v_rcp_f32 v32, v32\n\t"
            "v_rcp_f32 v33, v33\n\t"
            "v_pk_mul_f32 v[2:3], v[2:3], v[30:31]\n\t"
            "s_nop 0\n\t"
            "v_pk_mul_f32 v[4:5], v[4:5], v[32:33]\n\t"
            "v_cvt_pk_bf16_f32 v30, v2, v3\n\t"
            "v_cvt_pk_bf16_f32 v31, v4, v5\n\t"
            "s_mov_b32 s52, 0xffff0000\n\t"
            "s_mov_b32 s53, 0\n\t"
            "s_mov_b32 s54, 0\n\t"
            "s_mov_b32 s55, 0x0000ffff\n\t"
            "s_mov_b32 s56, 0\n\t"
            "s_mov_b32 s57, 0xffff0000\n\t"
            "s_waitcnt lgkmcnt(0)\n\t"
            "v_mfma_f32_16x16x16_bf16 v[32:35], v[30:31], v[6:7], 0\n\t"
            "v_mfma_f32_16x16x16_bf16 v[2:5], v[30:31], v[8:9], 0\n\t"
            "v_mfma_f32_16x16x16_bf16 v[6:9], v[30:31], v[10:11], 0\n\t"
            "v_mfma_f32_16x16x16_bf16 v[10:13], v[30:31], v[12:13], 0\n\t"
            "s_nop 7\n\t"
            "v_cndmask_b32 v36, v32, v2, s[52:53]\n\t"
            "v_cndmask_b32 v36, v36, v6, s[54:55]\n\t"
            "v_cndmask_b32 v36, v36, v10, s[56:57]\n\t"
            "v_mfma_f32_16x16x16_bf16 v[32:35], v[30:31], v[14:15], 0\n\t"
            "v_mfma_f32_16x16x16_bf16 v[2:5], v[30:31], v[16:17], 0\n\t"
            "v_mfma_f32_16x16x16_bf16 v[6:9], v[30:31], v[18:19], 0\n\t"
            "v_mfma_f32_16x16x16_bf16 v[18:21], v[30:31], v[20:21], 0\n\t"
            "v_mfma_f32_16x16x16_bf16 v[10:13], v[30:31], v[22:23], 0\n\t"
            "v_mfma_f32_16x16x16_bf16 v[22:25], v[30:31], v[24:25], 0\n\t"
            "v_mfma_f32_16x16x16_bf16 v[14:17], v[30:31], v[26:27], 0\n\t"
            "v_mfma_f32_16x16x16_bf16 v[26:29], v[30:31], v[28:29], 0\n\t"
            "s_nop 3\n\t"
            "v_cndmask_b32 v37, v32, v2, s[52:53]\n\t"
            "v_cndmask_b32 v37, v37, v6, s[54:55]\n\t"
            "v_cndmask_b32 v37, v37, v18, s[56:57]\n\t"
            "s_nop 0\n\t"
            "v_cndmask_b32 v30, v10, v22, s[52:53]\n\t"
            "v_cndmask_b32 v30, v30, v14, s[54:55]\n\t"
            "v_cndmask_b32 v30, v30, v26, s[56:57]\n\t"
            "v_cvt_pk_bf16_f32 v2, v36, v37\n\t"
            "v_cvt_pk_bf16_f32 v3, v30, v30\n\t"
            "v_lshlrev_b32 v4, 16, v2\n\t"
            "v_and_b32 v5, s50, v2\n\t"
            "v_lshlrev_b32 v6, 16, v3\n\t"
            "v_mul_f32 v4, 0xbfb8aa3b, v4\n\t"
            "v_mul_f32 v5, 0xbfb8aa3b, v5\n\t"
            "v_mul_f32 v6, 0xbfb8aa3b, v6\n\t"
            "v_exp_f32 v4, v4\n\t"
            "v_exp_f32 v5, v5\n\t"
            "v_exp_f32 v6, v6\n\t"
            "v_lshlrev_b32 v7, 2, %[lane]\n\t"
            "v_add_f32 v4, 1.0, v4\n\t"
            "v_add_f32 v5, 1.0, v5\n\t"
            "v_add_f32 v6, 1.0, v6\n\t"
            "v_rcp_f32 v4, v4\n\t"
            "v_rcp_f32 v5, v5\n\t"
            "v_rcp_f32 v6, v6\n\t"
            "v_add_u32 v7, %[lsc], v7\n\t"
            "v_cvt_pk_bf16_f32 v2, v4, v5\n\t"
            "v_cvt_pk_bf16_f32 v3, v6, v6\n\t"
            "v_lshlrev_b32 v4, 16, v2\n\t"
            "v_and_b32 v5, s50, v2\n\t"
            "v_lshlrev_b32 v6, 16, v3\n\t"
            "s_cmp_eq_u32 s49, 0\n\t"
            "s_cbranch_scc1 3f\n\t"
            "v_mov_b32 v4, 0x7fc00000\n\t"
            "v_mov_b32 v5, 0x7fc00000\n\t"
            "v_mov_b32 v6, 0x7fc00000\n"
            "3:\n\t"
            "ds_write2st64_b32 v7, v4, v5 offset0:0 offset1:1\n\t"
            "ds_write_b32 v7, v6 offset:512\n\t"
            "s_waitcnt lgkmcnt(0)"
            :
            : [lane] "v"(lane), [moff] "s"(WS_MEAN + (b % NREP) * MEAN_REP), [nmean] "i"(NMEAN), [mstr] "i"(MSTRIDE), [rs] "s"(rs_ws), [ta] "s"(ta), [tb] "s"(tb), [cap] "s"(kSpinCap),
              [lds] "s"((unsigned)(uintptr_t)L), [lsc] "s"((unsigned)(uintptr_t)(L + L_SCALE)),
              [lmean] "i"(L_MEAN), [lwd] "i"(L_WD), [lwu] "i"(L_WU)
            : "v2", "v3", "v4", "v5", "v6", "v7", "v8", "v9", "v10", "v11", "v12", "v13", "v14", "v15", "v16", "v17", "v18", "v19",
              "v20", "v21", "v22", "v23", "v24", "v25", "v26", "v27", "v28", "v29", "v30", "v31", "v32", "v33", "v34", "v35", "v36", "v37",
              "s48", "s49", "s50", "s52", "s53", "s54", "s55", "s56", "s57", "vcc", "scc", "memory");
        STAMP_AT(7);
    } else if (w == NPOLL + 1 && b == 0) {
        for (unsigned spins = 0; spins < kSpinCap; ++spins) {
            asm volatile("" ::: "memory");
            if (__all(granule_ok(__builtin_amdgcn_raw_buffer_load_b128(rs_ws, WS_MEAN, 0, AUX_SC1), ta, tb))) {
                if (lane == 0) __builtin_amdgcn_raw_buffer_store_b64(v2u{ta, tb}, rs_ws, WS_EPOCH, 0, AUX_SC1);
                break;
            }
            __builtin_amdgcn_s_sleep(2);
        }
    }
    __syncthreads();
    if (w == 0) STAMP_AT(8);

    {
        const f32x4 s0 = *(const LAS f32x4*)(L + L_SCALE + (seg * 64 + (lane & 7) * 8) * 4);
        const f32x4 s1 = *(const LAS f32x4*)(L + L_SCALE + (seg * 64 + (lane & 7) * 8) * 4 + 16);
        const f32x8 sc = {s0.x, s0.y, s0.z, s0.w, s1.x, s1.y, s1.z, s1.w};
        const __amdgpu_buffer_rsrc_t rs_o = __builtin_amdgcn_make_buffer_rsrc((unsigned char*)out + base, 0, (int)bytes, RSRC_W3);
        asm volatile(
            "s_mov_b32 s42, 0\n\t"
            "s_mov_b32 s43, 0\n\t"
            "s_mov_b32 s44, 0xffff0000\n\t"
            "s_mov_b32 s45, 0\n"
            "1:\n\t"
            "s_set_gpr_idx_on s42, gpr_idx(SRC1)\n\t"
            "v_lshlrev_b32 v16, 16, v40\n\t"
            "v_and_b32 v17, s44, v40\n\t"
            "v_lshlrev_b32 v18, 16, v41\n\t"
            "v_and_b32 v19, s44, v41\n\t"
            "v_lshlrev_b32 v20, 16, v42\n\t"
            "v_and_b32 v21, s44, v42\n\t"
            "v_lshlrev_b32 v22, 16, v43\n\t"
            "v_and_b32 v23, s44, v43\n\t"
            "s_set_gpr_idx_off\n\t"
            "v_pk_mul_f32 v[16:17], v[16:17], v[8:9]\n\t"
            "v_pk_mul_f32 v[18:19], v[18:19], v[10:11]\n\t"
            "v_pk_mul_f32 v[20:21], v[20:21], v[12:13]\n\t"
            "v_pk_mul_f32 v[22:23], v[22:23], v[14:15]\n\t"
            "v_cvt_pk_bf16_f32 v24, v16, v17\n\t"
            "v_cvt_pk_bf16_f32 v25, v18, v19\n\t"
            "v_cvt_pk_bf16_f32 v26, v20, v21\n\t"
            "v_cvt_pk_bf16_f32 v27, v22, v23\n\t"
            ".if %c[dt]\n\t"
            "s_cmp_eq_u32 s42, 0\n\t"
            "s_cbranch_scc1 4f\n"
            "2:\n\t"
            "s_memrealtime s[46:47]\n\t"
            "s_waitcnt lgkmcnt(0)\n\t"
            "s_sub_u32 s46, s46, s40\n\t"
            "s_cmp_ge_u32 s46, s45\n\t"
            "s_cbranch_scc1 3f\n\t"
            "s_sleep 1\n\t"
            "s_branch 2b\n"
            "4:\n\t"
            "s_memrealtime s[40:41]\n"
            "3:\n\t"
            ".endif\n\t"
            "buffer_store_dwordx4 v[24:27], %[voff], %[rs], s43 offen " ST_POL "\n\t"
            "s_add_u32 s43, s43, %[jstr]\n\t"
            "s_add_u32 s45, s45, %c[dt]\n\t"
            "s_add_u32 s42, s42, 4\n\t"
            "s_cmp_lt_u32 s42, 128\n\t"
            "s_cbranch_scc1 1b\n\t"
            :
            : "{v[40:71]}"(D[0]), "{v[72:103]}"(D[1]), "{v[104:135]}"(D[2]), "{v[136:167]}"(D[3]), "{v[8:15]}"(sc),
              [voff] "v"(voff), [rs] "s"(rs_o), [jstr] "i"(JSTR), [dt] "i"(PACE_ST)
            : "s40", "s41", "s42", "s43", "s44", "s45", "s46", "s47", "scc", "memory",
              "v16", "v17", "v18", "v19", "v20", "v21", "v22", "v23", "v24", "v25", "v26", "v27");
    }
}

constexpr int FB_WG = 512;
__global__ void __launch_bounds__(C) se_partial_kernel(const unsigned short* __restrict__ x, float* __restrict__ part, int npix) {
    float a = 0.f;
    for (int p = blockIdx.x; p < npix; p += gridDim.x) a += bf_lo(x[(size_t)p * C + threadIdx.x]);
    part[blockIdx.x * C + threadIdx.x] = a;
}
__global__ void __launch_bounds__(C) se_gate_kernel(const float* __restrict__ part, const unsigned short* __restrict__ wd,
                                                    const unsigned short* __restrict__ wu, float* __restrict__ scale, int npix) {
    __shared__ float m[C], h[R];
    const int c = threadIdx.x;
    float a = 0.f;
    for (int g = 0; g < FB_WG; ++g) a += part[g * C + c];
    m[c] = round_bf(a / (float)npix);
    __syncthreads();
    if (c < R) {
        float v = 0.f;
        for (int k = 0; k < C; ++k) v += m[k] * bf_lo(wd[k * R + c]);
        v = round_bf(v);
        h[c] = round_bf(v / (1.0f + __expf(-v)));
    }
    __syncthreads();
    float z = 0.f;
    for (int j = 0; j < R; ++j) z += h[j] * bf_lo(wu[j * C + c]);
    scale[c] = round_bf(1.0f / (1.0f + __expf(-round_bf(z))));
}
__global__ void __launch_bounds__(C) se_scale_kernel(const unsigned short* __restrict__ x, const float* __restrict__ scale,
                                                     unsigned short* __restrict__ out, int npix) {
    const float s = scale[threadIdx.x];
    for (int p = blockIdx.x; p < npix; p += gridDim.x) {
        const size_t i = (size_t)p * C + threadIdx.x;
        out[i] = (unsigned short)pack_bf(bf_lo(x[i]) * s, 0.f);
    }
}

}

extern "C" void kernel_launch(void* const* d_in, const int* in_sizes, int n_in, void* d_out, int out_size, void* d_ws,
                              size_t ws_size, hipStream_t stream) {
    (void)n_in; (void)out_size; (void)ws_size;
    const unsigned short* x = (const unsigned short*)d_in[0];
    const unsigned short* wd = (const unsigned short*)d_in[1];
    const unsigned short* wu = (const unsigned short*)d_in[2];
    unsigned short* out = (unsigned short*)d_out;
    int dev = 0, cus = 0, per_cu = 0;
    (void)hipGetDevice(&dev);
    (void)hipDeviceGetAttribute(&cus, hipDeviceAttributeMultiprocessorCount, dev);
    (void)hipOccupancyMaxActiveBlocksPerMultiprocessor(&per_cu, se_block_kernel, NT, 0);
    if (!FORCE_FALLBACK && in_sizes[0] == HW * C && cus * per_cu >= NWG) {
        se_block_kernel<<<NWG, NT, 0, stream>>>(x, wd, wu, out, (unsigned char*)d_ws);
    } else {
        const int npix = in_sizes[0] / C;
        float* part = (float*)d_ws;
        float* scale = part + FB_WG * C;
        se_partial_kernel<<<FB_WG, C, 0, stream>>>(x, part, npix);
        se_gate_kernel<<<1, C, 0, stream>>>(part, wd, wu, scale, npix);
        se_scale_kernel<<<4096, C, 0, stream>>>(x, scale, out, npix);
    }
}
```

```cpp
#include <hip/hip_runtime.h>
#include <hip/hip_bf16.h>
#include <stdint.h>

namespace {

#define LAS __attribute__((address_space(3)))
typedef unsigned v4u __attribute__((ext_vector_type(4)));
typedef unsigned v2u __attribute__((ext_vector_type(2)));
typedef unsigned v32u __attribute__((ext_vector_type(32)));
typedef float f32x4 __attribute__((ext_vector_type(4)));
typedef float f32x2 __attribute__((ext_vector_type(2)));
typedef float f32x8 __attribute__((ext_vector_type(8)));
typedef __bf16 bf16x2 __attribute__((ext_vector_type(2)));

constexpr int AUX_LD = 2;
constexpr int JL = 2;
constexpr int LDWIN = 5;
constexpr int PACE_LD = 410;
constexpr int PACE_ST = 38;
constexpr int H1DELAY = 8;
constexpr int NREP = 8;
constexpr int MSTRIDE = 64;

constexpr int C = 192, R = 12;
constexpr int HW = 512 * 512;
constexpr int NWG = 256, NT = 768, NSTEP = 32;
constexpr unsigned XBYTES = (unsigned)HW * C * 2;
constexpr unsigned STEP = NT * 16;
static_assert(STEP * NSTEP * NWG == XBYTES, "x is dealt out exactly");
constexpr unsigned piece_off(int j) { return (j / JL) * (JL * STEP * NWG) + (j % JL) * STEP; }
constexpr int NRED = 24;
constexpr int NPOLL = 8;
constexpr int NMEAN = C / 4;
constexpr int RSRC_W3 = 0x00020000;
constexpr int AUX_SC1 = 16;

constexpr unsigned WS_EPOCH = 0;
constexpr unsigned WS_PART = 4096;
constexpr unsigned PART_ROW = NWG * 64 + 128;
constexpr unsigned WS_MEAN = WS_PART + NRED * PART_ROW;
constexpr unsigned MEAN_REP = NMEAN * MSTRIDE;
constexpr unsigned WS_BYTES = WS_MEAN + NREP * MEAN_REP;
constexpr unsigned kSpinCap = 1u << 19;

constexpr int L_WD = 0;
constexpr int L_WU = L_WD + C * 32;
constexpr int L_PART = L_WU + 16 * C * 2;
constexpr int L_MEAN = L_PART + 4 * C * 4;
constexpr int L_SCALE = L_MEAN + C * 2;
constexpr int L_RED = L_SCALE + C * 4;
constexpr int L_CNT = L_RED + NPOLL * 32;
constexpr int L_BYTES = L_CNT + 16;

__device__ __forceinline__ float bf_lo(unsigned u) { return __builtin_bit_cast(float, u << 16); }
__device__ __forceinline__ float bf_hi(unsigned u) { return __builtin_bit_cast(float, u & 0xffff0000u); }
__device__ __forceinline__ unsigned pack_bf(float lo, float hi) {
    const bf16x2 p = {(__bf16)lo, (__bf16)hi};
    return __builtin_bit_cast(unsigned, p);
}
__device__ __forceinline__ float round_bf(float x) { return bf_lo(pack_bf(x, 0.f)); }
__device__ __forceinline__ float f_of(unsigned u) { return __builtin_bit_cast(float, u); }
__device__ __forceinline__ unsigned u_of(float f) { return __builtin_bit_cast(unsigned, f); }

__device__ __forceinline__ float fold32(float a, float b) {
    const auto r = __builtin_amdgcn_permlane32_swap(u_of(a), u_of(b), false, false);
    return f_of(r[0]) + f_of(r[1]);
}
__device__ __forceinline__ float fold16(float a, float b) {
    const auto r = __builtin_amdgcn_permlane16_swap(u_of(a), u_of(b), false, false);
    return f_of(r[0]) + f_of(r[1]);
}
template <int N> __device__ __forceinline__ float add_ror(float x) {
    return x + f_of(__builtin_amdgcn_update_dpp(0u, u_of(x), 0x120 + N, 0xf, 0xf, true));
}

__device__ __forceinline__ v4u make_granule(unsigned v0, unsigned v1, unsigned ta, unsigned tb) {
    return v4u{v0, v1, ta ^ v1, tb ^ v0};
}
__device__ __forceinline__ bool granule_ok(v4u g, unsigned ta, unsigned tb) {
    return ((g.z ^ g.y) == ta) & ((g.w ^ g.x) == tb);
}

__device__ __forceinline__ bool arrive_last(LAS unsigned char* cnt, unsigned n, int lane) {
    unsigned old = 0;
    __builtin_amdgcn_fence(__ATOMIC_RELEASE, "workgroup");
    if (lane == 0) old = __hip_atomic_fetch_add((LAS unsigned*)cnt, 1u, __ATOMIC_RELAXED, __HIP_MEMORY_SCOPE_WORKGROUP);
    old = __builtin_amdgcn_readfirstlane(old);
    __builtin_amdgcn_fence(__ATOMIC_ACQUIRE, "workgroup");
    return old == n - 1;
}
__device__ __forceinline__ void wait_arrivals(LAS unsigned char* cnt, unsigned n) {
    for (unsigned spins = 0; spins < kSpinCap; ++spins) {
        if (__hip_atomic_load((LAS unsigned*)cnt, __ATOMIC_RELAXED, __HIP_MEMORY_SCOPE_WORKGROUP) >= n) break;
        __builtin_amdgcn_s_sleep(1);
    }
}
__device__ __forceinline__ void pace(unsigned t0, unsigned ticks) {
    while ((unsigned)__builtin_amdgcn_s_memrealtime() - t0 < ticks) __builtin_amdgcn_s_sleep(1);
}

__global__ void __launch_bounds__(NT, 3)
se_block_kernel(const unsigned short* __restrict__ x, const unsigned short* __restrict__ wd,
                const unsigned short* __restrict__ wu, unsigned short* __restrict__ out, unsigned char* __restrict__ ws) {
    __shared__ __attribute__((aligned(16))) unsigned char smem[L_BYTES];
    LAS unsigned char* L = (LAS unsigned char*)smem;
    const int tid = threadIdx.x, lane = tid & 63, w = __builtin_amdgcn_readfirstlane(tid >> 6), b = blockIdx.x;
    const int seg = w % 3, pb = w / 3;
    const unsigned long long t_start = __builtin_amdgcn_s_memrealtime();

    const __amdgpu_buffer_rsrc_t rs_ws = __builtin_amdgcn_make_buffer_rsrc(ws, 0, (int)WS_BYTES, RSRC_W3);
    const __amdgpu_buffer_rsrc_t rs_x = __builtin_amdgcn_make_buffer_rsrc((void*)x, 0, (int)XBYTES, RSRC_W3);
    const unsigned voff = (unsigned)b * (JL * STEP) + (unsigned)((pb * 8 + (lane >> 3)) * 384 + seg * 128 + (lane & 7) * 16);

    v32u D[4];
    v4u d[NSTEP];
#pragma unroll
    for (int j = 0; j < LDWIN; ++j) d[j] = __builtin_amdgcn_raw_buffer_load_b128(rs_x, voff, piece_off(j), AUX_LD);
    __builtin_amdgcn_sched_barrier(0);
    const v2u ep = __builtin_amdgcn_raw_buffer_load_b64(rs_ws, WS_EPOCH, 0, AUX_SC1);
    const v4u wa = __builtin_amdgcn_raw_buffer_load_b128(__builtin_amdgcn_make_buffer_rsrc((void*)wd, 0, C * R * 2, RSRC_W3), tid * 16, 0, 0);
    const v4u wb = __builtin_amdgcn_raw_buffer_load_b128(__builtin_amdgcn_make_buffer_rsrc((void*)wu, 0, C * R * 2, RSRC_W3), (tid - 288) * 16, 0, 0);
    __builtin_amdgcn_sched_barrier(0);

    if (tid == 0) *(LAS v2u*)(L + L_CNT) = v2u{0u, 0u};

    asm volatile("" :: "s"(t_start));
    const unsigned t_ld = (unsigned)t_start;
    unsigned ta = 0, tb = 0;
    float acc[8];
#pragma unroll
    for (int e = 0; e < 8; ++e) acc[e] = 0.f;
#pragma unroll
    for (int j = 0; j < NSTEP; ++j) {
#pragma unroll
        for (int k = 0; k < 4; ++k) {
            acc[2 * k] += bf_lo(d[j][k]);
            acc[2 * k + 1] += bf_hi(d[j][k]);
            D[j / 8][4 * (j % 8) + k] = d[j][k];
        }
        if (j == LDWIN) {
            const v4u wv = wa | wb;
            const int g0 = 2 * tid, g1 = g0 + 1, t2 = tid - 288, t3 = tid - 576;
            const int a0 = tid < 288 ? L_WD + (g0 + ((g0 * 0x5556) >> 16)) * 8 : tid < 576 ? L_WU + t2 * 16 : L_WD + t3 * 32 + 24;
            const int a1 = tid < 288 ? L_WD + (g1 + ((g1 * 0x5556) >> 16)) * 8 : tid < 576 ? L_WU + t2 * 16 + 8 : L_WU + R * C * 2 + t3 * 8;
            *(LAS v2u*)(L + a0) = v2u{wv.x, wv.y};
            *(LAS v2u*)(L + a1) = v2u{wv.z, wv.w};
            unsigned long long tag = (((unsigned long long)ep.y << 32) | ep.x) + 1ull;
            if ((unsigned)tag == 0u) tag += 1ull;
            ta = __builtin_amdgcn_readfirstlane((unsigned)tag);
            tb = __builtin_amdgcn_readfirstlane((unsigned)(tag >> 32));
        }
        if (j == NSTEP / 2) __syncthreads();
        asm volatile("" : "+v"(acc[0]), "+v"(acc[1]), "+v"(acc[2]), "+v"(acc[3]), "+v"(acc[4]), "+v"(acc[5]), "+v"(acc[6]), "+v"(acc[7]));
        if (j + LDWIN < NSTEP) {
            pace(t_ld, (j + LDWIN) * PACE_LD / 10);
            d[j + LDWIN] = __builtin_amdgcn_raw_buffer_load_b128(rs_x, voff, piece_off(j + LDWIN), AUX_LD);
            __builtin_amdgcn_sched_barrier(0);
        }
    }
    {
        const float u0 = fold32(acc[0], acc[4]), u1 = fold32(acc[1], acc[5]), u2 = fold32(acc[2], acc[6]), u3 = fold32(acc[3], acc[7]);
        const float v0 = fold16(u0, u2), v1 = fold16(u1, u3);
        const float t0 = add_ror<8>(v0), t1 = add_ror<8>(v1);
        const float r = (lane & 8) ? t1 : t0;
        const int ch = seg * 64 + (lane & 7) * 8 + ((lane >> 3) & 1) + 2 * ((lane >> 4) & 1) + 4 * (lane >> 5);
        *(LAS float*)(L + L_PART + (pb * C + ch) * 4) = r;
    }
    bool good = true;
    if (arrive_last(L + L_CNT, NT / 64, lane)) {
        const LAS f32x2* p = (const LAS f32x2*)(L + L_PART) + lane;
        const LAS f32x2* q = (const LAS f32x2*)(L + L_PART) + 64 + (lane & 31);
        const f32x2 s0 = (p[0] + p[C / 2]) + (p[C] + p[3 * C / 2]);
        const f32x2 s1 = (q[0] + q[C / 2]) + (q[C] + q[3 * C / 2]);
        const unsigned off = WS_PART + (lane >> 2) * PART_ROW + b * 64 + (lane & 3) * 16;
        __builtin_amdgcn_raw_buffer_store_b128(make_granule(u_of(s0.x), u_of(s0.y), ta, tb), rs_ws, off, 0, AUX_SC1);
        if (lane < 32)
            __builtin_amdgcn_raw_buffer_store_b128(make_granule(u_of(s1.x), u_of(s1.y), ta, tb), rs_ws, off, 16 * PART_ROW, AUX_SC1);
        if (lane == 0) __hip_atomic_fetch_add((LAS unsigned*)(L + L_CNT), 1u, __ATOMIC_RELAXED, __HIP_MEMORY_SCOPE_WORKGROUP);
    }
    if (w < NPOLL) {
        if (b < NRED) {
            wait_arrivals(L + L_CNT, NT / 64 + 1);
            __builtin_amdgcn_s_sleep(H1DELAY);
            const unsigned off = WS_PART + b * PART_ROW + w * 2048 + lane * 16;
            v4u g0 = {0u, 0u, 0u, 0u}, g1 = {0u, 0u, 0u, 0u};
            bool k0 = false, k1 = false;
            for (unsigned spins = 0;; ++spins) {
                asm volatile("" ::: "memory");
                if (!k0) g0 = __builtin_amdgcn_raw_buffer_load_b128(rs_ws, off, 0, AUX_SC1);
                if (!k1) g1 = __builtin_amdgcn_raw_buffer_load_b128(rs_ws, off, 1024, AUX_SC1);
                k0 = granule_ok(g0, ta, tb); k1 = granule_ok(g1, ta, tb);
                if (__all(k0 & k1)) break;
                if (spins > kSpinCap) { good = false; break; }
            }
            float s = fold32(f_of(g0.x) + f_of(g1.x), f_of(g0.y) + f_of(g1.y));
            s = fold16(s, s);
            s = add_ror<8>(s); s = add_ror<4>(s);
            if (!good) s = __builtin_nanf("");
            if ((lane & 28) == 0) *(LAS float*)(L + L_RED + (w * 8 + 2 * (lane & 3) + (lane >> 5)) * 4) = s;
            if (arrive_last(L + L_CNT + 4, NPOLL, lane)) {
                const LAS f32x4* p = (const LAS f32x4*)(L + L_RED) + (lane & 1);
                const f32x4 m = (((p[0] + p[2]) + (p[4] + p[6])) + ((p[8] + p[10]) + (p[12] + p[14]))) * (1.0f / HW);
                if (lane < 2 * NREP)
                    __builtin_amdgcn_raw_buffer_store_b128(make_granule(pack_bf(m.x, m.y), pack_bf(m.z, m.w), ta, tb), rs_ws,
                                                           WS_MEAN + (lane >> 1) * MEAN_REP + (2 * b + (lane & 1)) * MSTRIDE, 0, AUX_SC1);
            }
        }
    } else if (w == NPOLL) {
        wait_arrivals(L + L_CNT, NT / 64 + 1);
        __builtin_amdgcn_fence(__ATOMIC_ACQUIRE, "workgroup");
        asm volatile(
            "v_lshrrev_b32 v2, 4, %[lane]\n\t"
            "v_bfe_u32 v3, %[lane], 2, 2\n\t"
            "v_and_b32 v4, 3, %[lane]\n\t"
            "v_lshlrev_b32 v34, 8, v2\n\t"
            "v_lshl_or_b32 v34, v3, 5, v34\n\t"
            "v_lshl_or_b32 v34, v4, 3, v34\n\t"
            "v_add_u32 v34, %[lds], v34\n\t"
            "v_and_b32 v35, 48, %[lane]\n\t"
            "v_add_u32 v35, %[lds], v35\n\t"
            "v_lshlrev_b32 v36, 3, %[lane]\n\t"
            "v_add_u32 v36, %[lds], v36\n\t"
            "v_lshrrev_b32 v5, 2, %[lane]\n\t"
            "v_mul_u32_u24 v37, 0x180, v5\n\t"
            "v_lshl_add_u32 v37, v4, 3, v37\n\t"
            "v_add_u32 v37, %[lds], v37\n\t"
            "v_cmp_gt_u32 vcc, %[nmean], %[lane]\n\t"
            "v_mul_u32_u24 v29, %[mstr], %[lane]\n\t"
            "s_nop 0\n\t"
            "v_cndmask_b32 v29, 0, v29, vcc\n\t"
            "v_add_u32 v29, %[moff], v29\n\t"
            "s_mov_b32 s48, 0\n\t"
            "s_mov_b32 s49, 0\n\t"
            "s_mov_b32 s50, 0xffff0000\n\t"
            "s_mov_b32 s54, 0\n\t"
            "s_mov_b32 s55, 0x0000ffff\n\t"
            "s_mov_b32 s56, 0\n\t"
            "s_mov_b32 s57, 0xffff0000\n\t"
            "s_mov_b32 s51, 0\n"
            "1:\n\t"
            "buffer_load_dwordx4 v[30:33], v29, %[rs], 0 offen sc1\n\t"
            "s_add_u32 s48, s48, 1\n\t"
            "s_waitcnt vmcnt(0)\n\t"
            "v_xor_b32 v2, v32, v31\n\t"
            "v_xor_b32 v3, v33, v30\n\t"
            "v_cmp_eq_u32 vcc, %[ta], v2\n\t"
            "v_cmp_eq_u32 s[52:53], %[tb], v3\n\t"
            "s_and_b64 vcc, vcc, s[52:53]\n\t"
            "s_cmp_eq_u64 vcc, exec\n\t"
            "s_cbranch_scc1 2f\n\t"
            "s_cmp_lt_u32 s48, %[cap]\n\t"
            "s_cbranch_scc1 1b\n\t"
            "s_mov_b32 s49, 1\n"
            "2:\n\t"
            "ds_write_b64 v36, v[30:31] offset:%c[lmean]\n\t"
            "ds_read_b64_tr_b16 v[2:3], v34 offset:%c[lwd]\n\t"
            "ds_read_b64_tr_b16 v[4:5], v34 offset:%c[lwd]+128\n\t"
            "ds_read_b128 v[6:9], v35 offset:%c[lmean]\n\t"
            "ds_read_b64_tr_b16 v[10:11], v34 offset:%c[lwd]+1024\n\t"
            "ds_read_b64_tr_b16 v[12:13], v34 offset:%c[lwd]+1152\n\t"
            "ds_read_b128 v[14:17], v35 offset:%c[lmean]+64\n\t"
            "ds_read_b64_tr_b16 v[18:19], v34 offset:%c[lwd]+2048\n\t"
            "ds_read_b64_tr_b16 v[20:21], v34 offset:%c[lwd]+2176\n\t"
            "ds_read_b128 v[22:25], v35 offset:%c[lmean]+128\n\t"
            "ds_read_b64_tr_b16 v[26:27], v34 offset:%c[lwd]+3072\n\t"
            "ds_read_b64_tr_b16 v[28:29], v34 offset:%c[lwd]+3200\n\t"
            "ds_read_b128 v[30:33], v35 offset:%c[lmean]+192\n\t"
            "s_waitcnt lgkmcnt(9)\n\t"
            "v_mfma_f32_16x16x32_bf16 v[2:5], v[2:5], v[6:9], 0\n\t"
            "ds_read_b64_tr_b16 v[6:7], v34 offset:%c[lwd]+4096\n\t"
            "ds_read_b64_tr_b16 v[8:9], v34 offset:%c[lwd]+4224\n\t"
            "s_waitcnt lgkmcnt(8)\n\t"
            "v_mfma_f32_16x16x32_bf16 v[2:5], v[10:13], v[14:17], v[2:5]\n\t"
            "ds_read_b128 v[10:13], v35 offset:%c[lmean]+256\n\t"
            "ds_read_b64_tr_b16 v[14:15], v34 offset:%c[lwd]+5120\n\t"
            "ds_read_b64_tr_b16 v[16:17], v34 offset:%c[lwd]+5248\n\t"
            "s_waitcnt lgkmcnt(8)\n\t"
            "v_mfma_f32_16x16x32_bf16 v[2:5], v[18:21], v[22:25], v[2:5]\n\t"
            "ds_read_b128 v[18:21], v35 offset:%c[lmean]+320\n\t"
            "s_waitcnt lgkmcnt(6)\n\t"
            "v_mfma_f32_16x16x32_bf16 v[2:5], v[26:29], v[30:33], v[2:5]\n\t"
            "s_waitcnt lgkmcnt(3)\n\t"
            "v_mfma_f32_16x16x32_bf16 v[2:5], v[6:9], v[10:13], v[2:5]\n\t"
            "s_waitcnt lgkmcnt(0)\n\t"
            "v_mfma_f32_16x16x32_bf16 v[2:5], v[14:17], v[18:21], v[2:5]\n\t"
            "ds_read_b64_tr_b16 v[6:7], v37 offset:%c[lwu]\n\t"
            "ds_read_b64_tr_b16 v[8:9], v37 offset:%c[lwu]+32\n\t"
            "ds_read_b64_tr_b16 v[10:11], v37 offset:%c[lwu]+64\n\t"
            "ds_read_b64_tr_b16 v[12:13], v37 offset:%c[lwu]+96\n\t"
            "ds_read_b64_tr_b16 v[22:23], v37 offset:%c[lwu]+256\n\t"
            "ds_read_b64_tr_b16 v[24:25], v37 offset:%c[lwu]+288\n\t"
            "ds_read_b64_tr_b16 v[26:27], v37 offset:%c[lwu]+320\n\t"
            "ds_read_b64_tr_b16 v[28:29], v37 offset:%c[lwu]+352\n\t"
            "ds_read_b64_tr_b16 v[14:15], v37 offset:%c[lwu]+128\n\t"
            "ds_read_b64_tr_b16 v[16:17], v37 offset:%c[lwu]+160\n\t"
            "ds_read_b64_tr_b16 v[18:19], v37 offset:%c[lwu]+192\n\t"
            "ds_read_b64_tr_b16 v[20:21], v37 offset:%c[lwu]+224\n\t"
            "v_cvt_pk_bf16_f32 v30, v2, v3\n\t"
            "v_cvt_pk_bf16_f32 v31, v4, v5\n\t"
            "v_lshlrev_b32 v2, 16, v30\n\t"
            "v_and_b32 v3, s50, v30\n\t"
            "v_lshlrev_b32 v4, 16, v31\n\t"
            "v_and_b32 v5, s50, v31\n\t"
            "v_mul_f32 v30, 0xbfb8aa3b, v2\n\t"
            "v_mul_f32 v31, 0xbfb8aa3b, v3\n\t"
            "v_mul_f32 v32, 0xbfb8aa3b, v4\n\t"
            "v_mul_f32 v33, 0xbfb8aa3b, v5\n\t"
            "v_exp_f32 v30, v30\n\t"
            "v_exp_f32 v31, v31\n\t"
            "v_exp_f32 v32, v32\n\t"
            "v_exp_f32 v33, v33\n\t"
            "v_add_f32 v30, 1.0, v30\n\t"
            "v_add_f32 v31, 1.0, v31\n\t"
            "v_add_f32 v32, 1.0, v32\n\t"
            "v_add_f32 v33, 1.0, v33\n\t"
            "v_rcp_f32 v30, v30\n\t"
            "v_rcp_f32 v31, v31\n\t"
            "v_rcp_f32 v32, v32\n\t"
            "v_rcp_f32 v33, v33\n\t"
            "v_pk_mul_f32 v[2:3], v[2:3], v[30:31]\n\t"
            "s_nop 0\n\t"
            "v_pk_mul_f32 v[4:5], v[4:5], v[32:33]\n\t"
            "v_cvt_pk_bf16_f32 v30, v2, v3\n\t"
            "v_cvt_pk_bf16_f32 v31, v4, v5\n\t"
            "v_cndmask_b32 v30, v30, 0, s[56:57]\n\t"
            "v_cndmask_b32 v31, v31, 0, s[56:57]\n\t"
            "s_waitcnt lgkmcnt(0)\n\t"
            "s_nop 1\n\t"
            "v_mfma_f32_16x16x16_bf16 v[32:35], v[30:31], v[6:7], 0\n\t"
            "v_mfma_f32_16x16x16_bf16 v[2:5], v[30:31], v[8:9], 0\n\t"
            "v_mfma_f32_16x16x16_bf16 v[6:9], v[30:31], v[10:11], 0\n\t"
            "v_mfma_f32_16x16x16_bf16 v[10:13], v[30:31], v[12:13], 0\n\t"
            "s_nop 7\n\t"
            "v_cndmask_b32 v36, v32, v2, s[50:51]\n\t"
            "v_cndmask_b32 v36, v36, v6, s[54:55]\n\t"
            "v_cndmask_b32 v36, v36, v10, s[56:57]\n\t"
            "v_mfma_f32_16x16x16_bf16 v[32:35], v[30:31], v[14:15], 0\n\t"
            "v_mfma_f32_16x16x16_bf16 v[2:5], v[30:31], v[16:17], 0\n\t"
            "v_mfma_f32_16x16x16_bf16 v[6:9], v[30:31], v[18:19], 0\n\t"
            "v_mfma_f32_16x16x16_bf16 v[18:21], v[30:31], v[20:21], 0\n\t"
            "v_mfma_f32_16x16x16_bf16 v[10:13], v[30:31], v[22:23], 0\n\t"
            "v_mfma_f32_16x16x16_bf16 v[22:25], v[30:31], v[24:25], 0\n\t"
            "v_mfma_f32_16x16x16_bf16 v[14:17], v[30:31], v[26:27], 0\n\t"
            "v_mfma_f32_16x16x16_bf16 v[26:29], v[30:31], v[28:29], 0\n\t"
            "s_nop 3\n\t"
            "v_cndmask_b32 v37, v32, v2, s[50:51]\n\t"
            "v_cndmask_b32 v37, v37, v6, s[54:55]\n\t"
            "v_cndmask_b32 v37, v37, v18, s[56:57]\n\t"
            "s_nop 0\n\t"
            "v_cndmask_b32 v30, v10, v22, s[50:51]\n\t"
            "v_cndmask_b32 v30, v30, v14, s[54:55]\n\t"
            "v_cndmask_b32 v30, v30, v26, s[56:57]\n\t"
            "v_cvt_pk_bf16_f32 v2, v36, v37\n\t"
            "v_cvt_pk_bf16_f32 v3, v30, v30\n\t"
            "v_lshlrev_b32 v4, 16, v2\n\t"
            "v_and_b32 v5, s50, v2\n\t"
            "v_lshlrev_b32 v6, 16, v3\n\t"
            "v_mul_f32 v4, 0xbfb8aa3b, v4\n\t"
            "v_mul_f32 v5, 0xbfb8aa3b, v5\n\t"
            "v_mul_f32 v6, 0xbfb8aa3b, v6\n\t"
            "v_exp_f32 v4, v4\n\t"
            "v_exp_f32 v5, v5\n\t"
            "v_exp_f32 v6, v6\n\t"
            "v_lshlrev_b32 v7, 2, %[lane]\n\t"
            "v_add_f32 v4, 1.0, v4\n\t"
            "v_add_f32 v5, 1.0, v5\n\t"
            "v_add_f32 v6, 1.0, v6\n\t"
            "v_rcp_f32 v4, v4\n\t"
            "v_rcp_f32 v5, v5\n\t"
            "v_rcp_f32 v6, v6\n\t"
            "v_add_u32 v7, %[lsc], v7\n\t"
            "v_cvt_pk_bf16_f32 v2, v4, v5\n\t"
            "v_cvt_pk_bf16_f32 v3, v6, v6\n\t"
            "v_lshlrev_b32 v4, 16, v2\n\t"
            "v_and_b32 v5, s50, v2\n\t"
            "v_lshlrev_b32 v6, 16, v3\n\t"
            "s_cmp_eq_u32 s49, 0\n\t"
            "s_cbranch_scc1 3f\n\t"
            "v_mov_b32 v4, 0x7fc00000\n\t"
            "v_mov_b32 v5, 0x7fc00000\n\t"
            "v_mov_b32 v6, 0x7fc00000\n"
            "3:\n\t"
            "ds_write2st64_b32 v7, v4, v5 offset0:0 offset1:1\n\t"
            "ds_write_b32 v7, v6 offset:512\n\t"
            "s_waitcnt lgkmcnt(0)"
            :
            : [lane] "v"(lane), [moff] "s"(WS_MEAN + (b % NREP) * MEAN_REP), [nmean] "i"(NMEAN), [mstr] "i"(MSTRIDE), [rs] "s"(rs_ws), [ta] "s"(ta), [tb] "s"(tb), [cap] "s"(kSpinCap),
              [lds] "s"((unsigned)(uintptr_t)L), [lsc] "s"((unsigned)(uintptr_t)(L + L_SCALE)),
              [lmean] "i"(L_MEAN), [lwd] "i"(L_WD), [lwu] "i"(L_WU)
            : "v2", "v3", "v4", "v5", "v6", "v7", "v8", "v9", "v10", "v11", "v12", "v13", "v14", "v15", "v16", "v17", "v18", "v19",
              "v20", "v21", "v22", "v23", "v24", "v25", "v26", "v27", "v28", "v29", "v30", "v31", "v32", "v33", "v34", "v35", "v36", "v37",
              "s48", "s49", "s50", "s51", "s52", "s53", "s54", "s55", "s56", "s57", "vcc", "scc", "memory");
    } else if (w == NPOLL + 1 && b == 0) {
        for (unsigned spins = 0; spins < kSpinCap; ++spins) {
            asm volatile("" ::: "memory");
            if (__all(granule_ok(__builtin_amdgcn_raw_buffer_load_b128(rs_ws, WS_MEAN, 0, AUX_SC1), ta, tb))) {
                if (lane == 0) __builtin_amdgcn_raw_buffer_store_b64(v2u{ta, tb}, rs_ws, WS_EPOCH, 0, AUX_SC1);
                break;
            }
            __builtin_amdgcn_s_sleep(2);
        }
    }
    __syncthreads();

    {
        const f32x4 s0 = *(const LAS f32x4*)(L + L_SCALE + (seg * 64 + (lane & 7) * 8) * 4);
        const f32x4 s1 = *(const LAS f32x4*)(L + L_SCALE + (seg * 64 + (lane & 7) * 8) * 4 + 16);
        const f32x8 sc = {s0.x, s0.y, s0.z, s0.w, s1.x, s1.y, s1.z, s1.w};
        const __amdgpu_buffer_rsrc_t rs_o = __builtin_amdgcn_make_buffer_rsrc((void*)out, 0, (int)XBYTES, RSRC_W3);
        asm volatile(
            "s_mov_b32 s42, 0\n\t"
            "s_mov_b32 s43, 0\n\t"
            "s_mov_b32 s44, 0xffff0000\n\t"
            "s_mov_b32 s45, 0\n"
            "1:\n\t"
            "s_set_gpr_idx_on s42, gpr_idx(SRC1)\n\t"
            "v_lshlrev_b32 v16, 16, v40\n\t"
            "v_and_b32 v17, s44, v40\n\t"
            "v_lshlrev_b32 v18, 16, v41\n\t"
            "v_and_b32 v19, s44, v41\n\t"
            "v_lshlrev_b32 v20, 16, v42\n\t"
            "v_and_b32 v21, s44, v42\n\t"
            "v_lshlrev_b32 v22, 16, v43\n\t"
            "v_and_b32 v23, s44, v43\n\t"
            "s_set_gpr_idx_off\n\t"
            "v_pk_mul_f32 v[16:17], v[16:17], v[8:9]\n\t"
            "v_pk_mul_f32 v[18:19], v[18:19], v[10:11]\n\t"
            "v_pk_mul_f32 v[20:21], v[20:21], v[12:13]\n\t"
            "v_pk_mul_f32 v[22:23], v[22:23], v[14:15]\n\t"
            "v_cvt_pk_bf16_f32 v24, v16, v17\n\t"
            "v_cvt_pk_bf16_f32 v25, v18, v19\n\t"
            "v_cvt_pk_bf16_f32 v26, v20, v21\n\t"
            "v_cvt_pk_bf16_f32 v27, v22, v23\n\t"
            "s_cmp_eq_u32 s42, 0\n\t"
            "s_cbranch_scc1 4f\n"
            "2:\n\t"
            "s_memrealtime s[46:47]\n\t"
            "s_waitcnt lgkmcnt(0)\n\t"
            "s_sub_u32 s46, s46, s40\n\t"
            "s_cmp_ge_u32 s46, s45\n\t"
            "s_cbranch_scc1 3f\n\t"
            "s_sleep 1\n\t"
            "s_branch 2b\n"
            "4:\n\t"
            "s_memrealtime s[40:41]\n"
            "3:\n\t"
            "buffer_store_dwordx4 v[24:27], %[voff], %[rs], s43 offen sc1\n\t"
            "s_add_u32 s47, s42, 4\n\t"
            "s_and_b32 s47, s47, 4 * %c[jl] - 1\n\t"
            "s_cselect_b32 s47, %[step], %[jump]\n\t"
            "s_add_u32 s43, s43, s47\n\t"
            "s_add_u32 s45, s45, %c[dt]\n\t"
            "s_add_u32 s42, s42, 4\n\t"
            "s_cmp_lt_u32 s42, 128\n\t"
            "s_cbranch_scc1 1b\n\t"
            :
            : "{v[40:71]}"(D[0]), "{v[72:103]}"(D[1]), "{v[104:135]}"(D[2]), "{v[136:167]}"(D[3]), "{v[8:15]}"(sc),
              [voff] "v"(voff), [rs] "s"(rs_o), [jl] "i"(JL), [step] "s"(STEP), [jump] "s"(JL * STEP * NWG - (JL - 1) * STEP), [dt] "i"(PACE_ST)
            : "s40", "s41", "s42", "s43", "s44", "s45", "s46", "s47", "scc", "memory",
              "v16", "v17", "v18", "v19", "v20", "v21", "v22", "v23", "v24", "v25", "v26", "v27");
    }
}

constexpr int FB_WG = 512;
__global__ void __launch_bounds__(C) se_partial_kernel(const unsigned short* __restrict__ x, float* __restrict__ part, int npix) {
    float a = 0.f;
    for (int p = blockIdx.x; p < npix; p += gridDim.x) a += bf_lo(x[(size_t)p * C + threadIdx.x]);
    part[blockIdx.x * C + threadIdx.x] = a;
}
__global__ void __launch_bounds__(C) se_gate_kernel(const float* __restrict__ part, const unsigned short* __restrict__ wd,
                                                    const unsigned short* __restrict__ wu, float* __restrict__ scale, int npix) {
    __shared__ float m[C], h[R];
    const int c = threadIdx.x;
    float a = 0.f;
    for (int g = 0; g < FB_WG; ++g) a += part[g * C + c];
    m[c] = round_bf(a / (float)npix);
    __syncthreads();
    if (c < R) {
        float v = 0.f;
        for (int k = 0; k < C; ++k) v += m[k] * bf_lo(wd[k * R + c]);
        v = round_bf(v);
        h[c] = round_bf(v / (1.0f + __expf(-v)));
    }
    __syncthreads();
    float z = 0.f;
    for (int j = 0; j < R; ++j) z += h[j] * bf_lo(wu[j * C + c]);
    scale[c] = round_bf(1.0f / (1.0f + __expf(-round_bf(z))));
}
__global__ void __launch_bounds__(C) se_scale_kernel(const unsigned short* __restrict__ x, const float* __restrict__ scale,
                                                     unsigned short* __restrict__ out, int npix) {
    const float s = scale[threadIdx.x];
    for (int p = blockIdx.x; p < npix; p += gridDim.x) {
        const size_t i = (size_t)p * C + threadIdx.x;
        out[i] = (unsigned short)pack_bf(bf_lo(x[i]) * s, 0.f);
    }
}

}

extern "C" void kernel_launch(void* const* d_in, const int* in_sizes, int n_in, void* d_out, int out_size, void* d_ws,
                              size_t ws_size, hipStream_t stream) {
    (void)n_in; (void)out_size; (void)ws_size;
    const unsigned short* x = (const unsigned short*)d_in[0];
    const unsigned short* wd = (const unsigned short*)d_in[1];
    const unsigned short* wu = (const unsigned short*)d_in[2];
    unsigned short* out = (unsigned short*)d_out;
    int dev = 0, cus = 0, per_cu = 0;
    (void)hipGetDevice(&dev);
    (void)hipDeviceGetAttribute(&cus, hipDeviceAttributeMultiprocessorCount, dev);
    (void)hipOccupancyMaxActiveBlocksPerMultiprocessor(&per_cu, se_block_kernel, NT, 0);
    if (in_sizes[0] == HW * C && cus * per_cu >= NWG) {
        se_block_kernel<<<NWG, NT, 0, stream>>>(x, wd, wu, out, (unsigned char*)d_ws);
    } else {
        const int npix = in_sizes[0] / C;
        float* part = (float*)d_ws;
        float* scale = part + FB_WG * C;
        se_partial_kernel<<<FB_WG, C, 0, stream>>>(x, part, npix);
        se_gate_kernel<<<1, C, 0, stream>>>(part, wd, wu, scale, npix);
        se_scale_kernel<<<4096, C, 0, stream>>>(x, scale, out, npix);
    }
}
```
